```python
import torch

def _softmax(x, axis=-1):
    return torch.softmax(x, dim=axis)
def _gelu(x):
    return torch.nn.functional.gelu(x, approximate="tanh")
def _silu(x):
    return torch.nn.functional.silu(x)
def _sigmoid(x):
    return torch.sigmoid(x)
def _layernorm(x, axis=-1, eps=1e-5):
    m = x.mean(dim=axis, keepdim=True)
    v = x.var(dim=axis, keepdim=True, unbiased=False)
    return (x - m) / torch.sqrt(v + eps)
def _scatter_add(tgt, idx, src, axis=0):
    return tgt.clone().index_add_(axis, idx, src)
def _segment_sum(src, seg, n):
    out = torch.zeros((n,) + src.shape[1:], dtype=src.dtype)
    return out.index_add_(0, seg, src)
def _pad(x, pw):
    flat = tuple(v for pair in reversed(pw) for v in pair)
    return torch.nn.functional.pad(x, flat)

kernel_name = 'recurrent_scan__sliding_block_cached__geglu_ffn__prompt'

def setup_inputs():

    _w = (lambda a: (lambda t: t.to(torch.bfloat16) if t.is_floating_point() else t)(torch.as_tensor(a)))
    def _A_setup():
        B, Sq, D = 1, 2048, 512
        Hq, Hkv, Sk, Dh = 32, 32, 2048, 128
        assert Hq % Hkv == 0, (Hq, Hkv)
        return {
            "x": _w((lambda *s: torch.randn(*s, dtype=torch.bfloat16) * 0.05)(B, Sq, D)),
            "k_cache": _w((lambda *s: torch.randn(*s, dtype=torch.bfloat16) * 0.8)(B, Hkv, Sk, Dh)),
            "v_cache": _w((lambda *s: torch.randn(*s, dtype=torch.bfloat16) * 0.2)(B, Hkv, Sk, Dh)),
            "wq": _w((lambda *s: torch.randn(*s, dtype=torch.bfloat16) * 0.8)(D, Hq * Dh) * (D ** -0.5)),
            "wo": _w((lambda *s: torch.randn(*s, dtype=torch.bfloat16) * 0.2)(Hq * Dh, D) * ((Hq * Dh) ** -0.5)),
        }
    _A_in = _A_setup()

    def _B_setup():
        B, S, D, F = 1, 2048, 512, 1376
        return {
            "x": _w((lambda *s: torch.randn(*s, dtype=torch.bfloat16) * 0.05)(B, S, D)),
            "w_gate": _w((lambda *s: torch.randn(*s, dtype=torch.bfloat16) * 0.1)(D, F)),
            "w_up": _w((lambda *s: torch.randn(*s, dtype=torch.bfloat16) * 0.1)(D, F)),
            "w_down": _w((lambda *s: torch.randn(*s, dtype=torch.bfloat16) * 0.1)(F, D)),
        }
    _B_in = _B_setup()

    return {
        "A_x": _A_in["x"],
        "A_k_cache": _A_in["k_cache"],
        "A_v_cache": _A_in["v_cache"],
        "A_wq": _A_in["wq"],
        "A_wo": _A_in["wo"],
        "B_x": _B_in["x"],
        "B_w_gate": _B_in["w_gate"],
        "B_w_up": _B_in["w_up"],
        "B_w_down": _B_in["w_down"],
    }

def reference(A_x, A_k_cache, A_v_cache, A_wq, A_wo, B_x, B_w_gate, B_w_up, B_w_down):
    def _A_ref(x, k_cache, v_cache, wq, wo):
        B, Sq, D = x.shape
        Hq, Dh = 32, 128
        Hkv, Sk = k_cache.shape[1], k_cache.shape[2]
        W = min(4096, max(256, Sk // 4))
        rep = Hq // Hkv
        q = (lambda x, *d: x.permute(*d))((x @ wq).reshape(B, Sq, Hq, Dh), 0, 2, 1, 3)
        k = (lambda x, n, axis: torch.repeat_interleave(x, n, dim=axis))(k_cache[:, :, Sk - W:, :], rep, axis=1)
        v = (lambda x, n, axis: torch.repeat_interleave(x, n, dim=axis))(v_cache[:, :, Sk - W:, :], rep, axis=1)
        scores = torch.einsum("bhqd,bhkd->bhqk", q, k) * (Dh ** -0.5)
        p = _softmax(scores, axis=-1)
        o = torch.einsum("bhqk,bhkd->bhqd", p, v)
        return (lambda x, *d: x.permute(*d))(o, 0, 2, 1, 3).reshape(B, Sq, Hq * Dh) @ wo

    def _B_ref(x, w_gate, w_up, w_down):
        return (_gelu(x @ w_gate) * (x @ w_up)) @ w_down

    x = A_x
    xn = _layernorm(x, axis=-1)
    s = _A_ref(xn, A_k_cache, A_v_cache, A_wq, A_wo)
    g = _silu(_B_ref(xn, B_w_gate, B_w_up, B_w_down))
    return x + s * g
```

```cpp
#include <hip/hip_runtime.h>
#include <hip/hip_bf16.h>
#include <stdint.h>

typedef __bf16 bf16x8 __attribute__((ext_vector_type(8)));
typedef __bf16 bf16x2 __attribute__((ext_vector_type(2)));
typedef short s16x4 __attribute__((ext_vector_type(4)));
typedef short s16x8 __attribute__((ext_vector_type(8)));
typedef float f32x16 __attribute__((ext_vector_type(16)));
typedef float f32x4 __attribute__((ext_vector_type(4)));
typedef float f32x2 __attribute__((ext_vector_type(2)));
typedef unsigned u32x4 __attribute__((ext_vector_type(4)));
typedef unsigned u32x2 __attribute__((ext_vector_type(2)));
#define LAS __attribute__((address_space(3)))
#define GAS __attribute__((address_space(1)))
#define DI __device__ __forceinline__

constexpr int SEQ = 2048, DM = 512, NH = 32, DH = 128, WIN = 512, KOFF = SEQ - WIN, FF = 1376;
constexpr int NFT = FF / 16;

DI void glds16(const void* g, unsigned ldsaddr) {
    asm volatile("s_mov_b32 m0, %0\n\ts_nop 0\n\tglobal_load_lds_dwordx4 %1, off" ::"s"(ldsaddr), "v"(g) : "memory");
}
DI void glds16s(const void* sbase, unsigned voff, unsigned ldsaddr) {
    asm volatile("s_mov_b32 m0, %0\n\ts_nop 0\n\tglobal_load_lds_dwordx4 %1, %2" ::"s"(ldsaddr), "v"(voff), "s"(sbase) : "memory");
}
#define WAITV(n) asm volatile("s_waitcnt vmcnt(" #n ")" ::: "memory")
#define WAITL0() asm volatile("s_waitcnt lgkmcnt(0)" ::: "memory")
#define BARRIER() do { WAITL0(); __builtin_amdgcn_s_barrier(); asm volatile("" ::: "memory"); __builtin_amdgcn_sched_barrier(0); } while (0)
DI unsigned lane_id_opaque() {
    unsigned l;
    asm volatile("v_mbcnt_lo_u32_b32 %0, -1, 0\n\tv_mbcnt_hi_u32_b32 %0, -1, %0" : "=v"(l));
    return l;
}

DI bf16x8 tr_frag(const char* lds, unsigned a0, unsigned a1) {
    s16x4 lo = __builtin_amdgcn_ds_read_tr16_b64_v4i16((LAS s16x4*)(lds + a0));
    s16x4 hi = __builtin_amdgcn_ds_read_tr16_b64_v4i16((LAS s16x4*)(lds + a1));
    s16x8 v = __builtin_shufflevector(lo, hi, 0, 1, 2, 3, 4, 5, 6, 7);
    return __builtin_bit_cast(bf16x8, v);
}
DI bf16x8 row_frag(const char* lds, unsigned a) { return *(const LAS bf16x8*)(lds + a); }
DI f32x16 mfma(bf16x8 a, bf16x8 b, f32x16 c) { return __builtin_amdgcn_mfma_f32_32x32x16_bf16(a, b, c, 0, 0, 0); }
DI unsigned cvtpk(float lo, float hi) {
    f32x2 v = {lo, hi};
    bf16x2 b = __builtin_convertvector(v, bf16x2);
    return __builtin_bit_cast(unsigned, b);
}
DI float bfr(float x) {
    return __uint_as_float(cvtpk(x, x) << 16);
}
DI float dot2(unsigned a, unsigned b, float c) {
    return __builtin_amdgcn_fdot2_f32_bf16(__builtin_bit_cast(bf16x2, a), __builtin_bit_cast(bf16x2, b), c, false);
}
DI float sum32(float x) {
    auto rr = __builtin_amdgcn_permlane32_swap(__float_as_uint(x), __float_as_uint(x), false, false);
    return __uint_as_float(rr[0]) + __uint_as_float(rr[1]);
}
DI float max32(float x) {
    auto rr = __builtin_amdgcn_permlane32_swap(__float_as_uint(x), __float_as_uint(x), false, false);
    return fmaxf(__uint_as_float(rr[0]), __uint_as_float(rr[1]));
}
DI bf16x8 pack8(const f32x16& x, int s) {
    u32x4 p = {cvtpk(x[8 * s + 0], x[8 * s + 1]), cvtpk(x[8 * s + 2], x[8 * s + 3]), cvtpk(x[8 * s + 4], x[8 * s + 5]), cvtpk(x[8 * s + 6], x[8 * s + 7])};
    return __builtin_bit_cast(bf16x8, p);
}

constexpr unsigned XB = 32768, WB = 28672, CB = XB + WB;
constexpr unsigned K1_MISC = 2 * CB + 32768;
constexpr unsigned K1_LDS = 163840;
DI constexpr unsigned kv_slot(int i) {
    int s = i & 7;
    return s < 2 ? 2 * CB + 16384u * s : (s < 5 ? 16384u * (s - 2) : CB + 16384u * (s - 5));
}

__global__ __launch_bounds__(512, 2) void k1_attn(const __hip_bfloat16* __restrict__ x, const __hip_bfloat16* __restrict__ kc,
                                                  const __hip_bfloat16* __restrict__ vc, const __hip_bfloat16* __restrict__ wq,
                                                  const __hip_bfloat16* __restrict__ wg, const __hip_bfloat16* __restrict__ wu,
                                                  u32x4* __restrict__ ofrag, u32x4* __restrict__ hfrag, unsigned* __restrict__ cnt) {
    extern __shared__ __attribute__((aligned(16))) char lds[];
    const int tid = threadIdx.x, lane = tid & 63;
    const int wave = __builtin_amdgcn_readfirstlane(tid >> 6);
    const int r = lane & 31, hh = lane >> 5;
    const int bid = blockIdx.x, xcd = bid & 7, idx = bid >> 3;
    const int head = xcd * 4 + (idx & 3), qb = idx >> 2;
    if (bid == 0 && tid < 64) cnt[tid * 16] = 0u;
    float* csum = (float*)(lds + K1_MISC);
    if (tid < 256) csum[tid] = 0.f;

    const unsigned ldsb = (unsigned)(size_t)(LAS char*)lds;
    const unsigned xoff0 = (lane >> 3) * (DM * 2) + ((lane & 7) ^ (lane >> 4)) * 16;
    const unsigned xoff1 = (xoff0 ^ 64) + 8 * (DM * 2);
    const char* xbase = (const char*)x + (size_t)(qb * 256 + wave * 32) * (DM * 2);
    const char* wsrc;
    unsigned wld;
    {
        const int krow = 8 * (lane >> 5) + ((lane >> 2) & 7), ch = lane & 3;
        if (wave < 4) {
            wld = NH * DH * 2;
            wsrc = (const char*)wq + (size_t)krow * wld + (head * DH + wave * 32 + ch * 8) * 2;
        } else {
            wld = FF * 2;
            int ft = head * 3 + (wave - 4);
            ft = ft < NFT ? ft : NFT - 1;
            const char* b = (ch < 2) ? (const char*)wg : (const char*)wu;
            wsrc = b + (size_t)krow * wld + (ft * 16 + (ch & 1) * 8) * 2;
        }
    }
    const unsigned koff = (wave * 8 + (lane >> 4)) * (DH * 2) + (((lane & 15) ^ ((wave * 8 + (lane >> 4)) & 15)) * 16);
    const unsigned voff = (wave * 8 + ((lane >> 2) & 7)) * (DH * 2) + ((lane >> 5) * 32 + (lane & 3) * 8) * 2;
    const char* kbase = (const char*)kc + ((size_t)head * SEQ + KOFF) * (DH * 2);
    const char* vbase = (const char*)vc + ((size_t)head * SEQ + KOFF) * (DH * 2);
    auto stage_chunk = [&](int c, unsigned buf) {
        const unsigned xo = ldsb + buf + wave * 4096;
        const char* s0 = xbase + c * 128;
        glds16s(s0, xoff0, xo);
        glds16s(s0, xoff1, xo + 1024);
        glds16s(s0 + 16 * (DM * 2), xoff0, xo + 2048);
        glds16s(s0 + 16 * (DM * 2), xoff1, xo + 3072);
        if (wave < 7) {
            const char* w0 = wsrc + (size_t)c * 64 * wld;
            const unsigned wo_ = ldsb + buf + XB + wave * 4096;
            glds16(w0, wo_);
            glds16(w0 + 16 * wld, wo_ + 1024);
            glds16(w0 + 32 * wld, wo_ + 2048);
            glds16(w0 + 48 * wld, wo_ + 3072);
        }
    };
    auto stage_kv = [&](int i) {
        const unsigned so = ldsb + kv_slot(i) + wave * 2048;
        const bool isk = (i == 0) || ((i & 1) && i < 15);
        if (isk) {
            const int j = (i + 1) >> 1;
            const char* s = kbase + (size_t)j * 64 * (DH * 2);
            glds16s(s, koff, so);
            glds16s(s + 4 * (DH * 2), koff ^ 64, so + 1024);
        } else {
            const int j = (i == 15) ? 7 : ((i - 2) >> 1);
            const char* s = vbase + (size_t)j * 64 * (DH * 2);
            glds16s(s, voff, so);
            glds16s(s + 128, voff, so + 1024);
        }
    };

    f32x16 acc[7];
#pragma unroll
    for (int t = 0; t < 7; ++t) acc[t] = f32x16{};
    float cs[4] = {0.f, 0.f, 0.f, 0.f};
    float ssum = 0.f, ssq = 0.f;
    const int q4 = (lane & 15) >> 2, p4 = lane & 3, g16 = (lane >> 4) & 1;
    const unsigned trN = hh * 512 + q4 * 64 + g16 * 32 + p4 * 8;
    const unsigned trS = hh * 512 + q4 * 64 + g16 * 32 + (((p4 & 1) << 1) | (p4 >> 1)) * 8;
    const unsigned xrd = (wave * 32 + r) * 128 + ((hh ^ ((r >> 1) & 7)) * 16);
    const unsigned csrd = ((wave >> 2) ? trN : trS) + (wave >> 2) * 16384 + (wave & 3) * 1024;
    const unsigned ONES = 0x3F803F80u;

    stage_chunk(0, 0);
    stage_kv(0);
    stage_kv(1);
    WAITV(0);
    BARRIER();
#pragma unroll 1
    for (int c = 0; c < 8; ++c) {
        const unsigned buf = (c & 1) ? CB : 0;
        if (c < 7) {
            stage_chunk(c + 1, buf ^ CB);
        } else {
            stage_kv(2);
            stage_kv(3);
            stage_kv(4);
        }
        const char* wb = lds + buf + XB;
        const char* xb = lds + buf;
#pragma unroll
        for (int ks = 0; ks < 4; ++ks) {
            bf16x8 b = row_frag(xb, xrd ^ (ks * 32));
            u32x4 bu = __builtin_bit_cast(u32x4, b);
#pragma unroll
            for (int e = 0; e < 4; ++e) {
                ssum = dot2(bu[e], ONES, ssum);
                ssq = dot2(bu[e], bu[e], ssq);
            }
#pragma unroll
            for (int t = 0; t < 7; ++t) {
                const unsigned a = (t < 4 ? trS : trN) + t * 4096 + ks * 1024;
                acc[t] = mfma(tr_frag(wb, a, a + 256), b, acc[t]);
            }
        }
#pragma unroll
        for (int i = 0; i < 4; ++i) {
            u32x4 au = __builtin_bit_cast(u32x4, tr_frag(wb, csrd + i * 4096, csrd + i * 4096 + 256));
#pragma unroll
            for (int e = 0; e < 4; ++e) cs[i] = dot2(au[e], ONES, cs[i]);
        }
        WAITV(0);
        BARRIER();
    }
    stage_kv(5);
    stage_kv(6);
    stage_kv(7);
#pragma unroll
    for (int i = 0; i < 4; ++i)
        if ((wave >> 2) * 4 + i < 7) atomicAdd(&csum[((wave >> 2) * 4 + i) * 32 + r], cs[i]);
    ssum = sum32(ssum);
    ssq = sum32(ssq);
    const float mean = ssum * (1.f / DM);
    const float var = fmaxf(ssq * (1.f / DM) - mean * mean, 0.f);
    const float mb = bfr(mean);
    const float den = bfr(__builtin_sqrtf(bfr(bfr(var) + 1e-5f)));
    const float rs = 1.f / den;
    BARRIER();
    bf16x8 qf[8];
    {
        const float qs = rs * (0.08838834764831845f * 1.4426950408889634f);
#pragma unroll
        for (int t = 0; t < 4; ++t) {
#pragma unroll
            for (int g = 0; g < 4; ++g) {
                f32x4 c4 = *(const f32x4*)(csum + t * 32 + 8 * g + 4 * hh);
#pragma unroll
                for (int e = 0; e < 4; ++e) acc[t][4 * g + e] = (acc[t][4 * g + e] - mb * c4[e]) * qs;
            }
            qf[2 * t] = pack8(acc[t], 0);
            qf[2 * t + 1] = pack8(acc[t], 1);
        }
#pragma unroll
        for (int t = 4; t < 7; ++t) {
#pragma unroll
            for (int g = 0; g < 4; ++g) {
                f32x4 c4 = *(const f32x4*)(csum + t * 32 + 8 * g + 4 * hh);
#pragma unroll
                for (int e = 0; e < 4; ++e) acc[t][4 * g + e] = (acc[t][4 * g + e] - mb * c4[e]) * rs;
            }
            f32x16 hv;
#pragma unroll
            for (int e = 0; e < 8; ++e) {
                const float g = acc[t][e], u = acc[t][8 + e];
                const float z = g * (1.f + 0.044715f * g * g) * (2.f * 0.7978845608028654f * 1.4426950408889634f);
                hv[e] = g * __builtin_amdgcn_rcpf(1.f + __builtin_amdgcn_exp2f(-z)) * u;
            }
            const int ft = head * 3 + (t - 4);
            if (ft < NFT) hfrag[((size_t)(qb * 8 + wave) * NFT + ft) * 64 + lane] = __builtin_bit_cast(u32x4, pack8(hv, 0));
        }
    }

    f32x16 o[4];
#pragma unroll
    for (int t = 0; t < 4; ++t) o[t] = f32x16{};
    float m = -1e30f, l = 0.f;
    unsigned krd[2];
    krd[0] = r * 256 + ((hh ^ (r & 15)) * 16);
    krd[1] = krd[0] + 8192;
    auto qk = [&](int j, f32x16& s0, f32x16& s1) {
        const char* kb = lds + kv_slot(j == 0 ? 0 : 2 * j - 1);
        s0 = f32x16{};
        s1 = f32x16{};
#pragma unroll
        for (int s = 0; s < 8; ++s) {
            s0 = mfma(row_frag(kb, krd[0] ^ (s * 32)), qf[s], s0);
            s1 = mfma(row_frag(kb, krd[1] ^ (s * 32)), qf[s], s1);
        }
    };
    auto softmax = [&](f32x16& s0, f32x16& s1, bf16x8 (&pf)[4]) {
        float pm = s0[0];
#pragma unroll
        for (int e = 1; e < 16; ++e) pm = fmaxf(pm, s0[e]);
#pragma unroll
        for (int e = 0; e < 16; ++e) pm = fmaxf(pm, s1[e]);
        pm = max32(pm);
        if (__builtin_expect(__any(pm > m + 8.f), 0)) {
            const float mn = fmaxf(m, pm);
            const float al = __builtin_amdgcn_exp2f(m - mn);
            m = mn;
            l *= al;
#pragma unroll
            for (int t = 0; t < 4; ++t)
#pragma unroll
                for (int e = 0; e < 16; ++e) o[t][e] *= al;
        }
        float ps = 0.f;
#pragma unroll
        for (int e = 0; e < 16; ++e) {
            s0[e] = __builtin_amdgcn_exp2f(s0[e] - m);
            ps += s0[e];
        }
#pragma unroll
        for (int e = 0; e < 16; ++e) {
            s1[e] = __builtin_amdgcn_exp2f(s1[e] - m);
            ps += s1[e];
        }
        l += ps;
        pf[0] = pack8(s0, 0);
        pf[1] = pack8(s0, 1);
        pf[2] = pack8(s1, 0);
        pf[3] = pack8(s1, 1);
    };
    auto pv = [&](int j, bf16x8 (&pf)[4]) {
        const char* vb = lds + kv_slot(j == 7 ? 15 : 2 * j + 2);
#pragma unroll
        for (int ks = 0; ks < 4; ++ks)
#pragma unroll
            for (int t = 0; t < 4; ++t) {
                const unsigned a = (hh * 4 + q4) * 64 + g16 * 32 + p4 * 8 + (2 * ks * 4 + t) * 512;
                o[t] = mfma(tr_frag(vb, a, a + 2048), pf[ks], o[t]);
            }
    };
    f32x16 sA0, sA1, sB0, sB1;
    bf16x8 pf[4];
    qk(0, sA0, sA1);
    qk(1, sB0, sB1); softmax(sA0, sA1, pf); pv(0, pf);
    BARRIER(); stage_kv(8); stage_kv(9); stage_kv(10);
    qk(2, sA0, sA1); softmax(sB0, sB1, pf); pv(1, pf);
    WAITV(8); BARRIER(); stage_kv(11); stage_kv(12);
    qk(3, sB0, sB1); softmax(sA0, sA1, pf); pv(2, pf);
    WAITV(8); BARRIER(); stage_kv(13); stage_kv(14);
    qk(4, sA0, sA1); softmax(sB0, sB1, pf); pv(3, pf);
    WAITV(8); BARRIER(); stage_kv(15);
    qk(5, sB0, sB1); softmax(sA0, sA1, pf); pv(4, pf);
    WAITV(6); BARRIER();
    qk(6, sA0, sA1); softmax(sB0, sB1, pf); pv(5, pf);
    WAITV(2); BARRIER();
    qk(7, sB0, sB1); softmax(sA0, sA1, pf); pv(6, pf);
    WAITV(0); BARRIER();
    softmax(sB0, sB1, pf); pv(7, pf);

    l = sum32(l);
    const float rl = 1.f / l;
    const unsigned lane2 = lane_id_opaque();
#pragma unroll
    for (int t = 0; t < 4; ++t) {
#pragma unroll
        for (int e = 0; e < 16; ++e) o[t][e] *= rl;
        u32x4* dst = ofrag + ((size_t)(qb * 8 + wave) * 256 + head * 8 + 2 * t) * 64 + lane2;
        dst[0] = __builtin_bit_cast(u32x4, pack8(o[t], 0));
        dst[64] = __builtin_bit_cast(u32x4, pack8(o[t], 1));
    }
}

constexpr unsigned K2_STG = 32768;
constexpr unsigned K2_LDS = 4 * K2_STG + 1024;

__global__ __launch_bounds__(512, 2) void k2_out(const __hip_bfloat16* __restrict__ x, const __hip_bfloat16* __restrict__ wo,
                                                 const __hip_bfloat16* __restrict__ wd, const u32x4* __restrict__ ofrag,
                                                 const u32x4* __restrict__ hfrag, float* __restrict__ part, unsigned* __restrict__ cnt,
                                                 __hip_bfloat16* __restrict__ out) {
    extern __shared__ __attribute__((aligned(16))) char lds[];
    const int tid = threadIdx.x, lane = tid & 63;
    const int wave = __builtin_amdgcn_readfirstlane(tid >> 6);
    const int r = lane & 31, hh = lane >> 5;
    const int bid = blockIdx.x, xcd = bid & 7, idx = bid >> 3;
    const int sp = xcd & 3, tq = (xcd >> 2) * 8 + (idx >> 2), tn = idx & 3;
    const int tile = tq * 4 + tn;
    const int g = wave & 3, kh = wave >> 2;
    const int s0 = sp < 3 ? sp * 86 : 0;
    const int ns = sp == 2 ? 84 : 86;
    const int ktot = sp < 3 ? NH * DH : FF;
    const char* wbase = (const char*)(sp < 3 ? wo : wd);
    const unsigned ldsb = (unsigned)(size_t)(LAS char*)lds;
    const char* bbase = (const char*)(sp < 3 ? ofrag + ((size_t)(tq * 4 + g) * 256 + s0) * 64 : hfrag + (size_t)(tq * 4 + g) * NFT * 64);
    const unsigned boff = lane * 16;
    const int wrow = s0 * 16 + wave * 16 + ((lane >> 2) & 7);
    const unsigned wcol = (tn * 128 + (lane >> 5) * 32 + (lane & 3) * 8) * 2;
    auto stage_w = [&](int c, unsigned buf) {
        int r0 = wrow + c * 128, r1 = r0 + 8;
        r0 = r0 < ktot ? r0 : ktot - 1;
        r1 = r1 < ktot ? r1 : ktot - 1;
        const unsigned a0 = r0 * (DM * 2) + wcol, a1 = r1 * (DM * 2) + wcol;
        const unsigned o_ = ldsb + buf + wave * 4096;
        glds16s(wbase, a0, o_);
        glds16s(wbase + 128, a0, o_ + 1024);
        glds16s(wbase, a1, o_ + 2048);
        glds16s(wbase + 128, a1, o_ + 3072);
    };
    f32x16 acc[4];
#pragma unroll
    for (int t = 0; t < 4; ++t) acc[t] = f32x16{};
    const int q4 = (lane & 15) >> 2, p4 = lane & 3, g16 = (lane >> 4) & 1;
    const unsigned trd = (hh * 4 + q4) * 64 + g16 * 32 + p4 * 8 + kh * 4096;
    u32x4 bf[3][4];
    auto load_b = [&](int c, u32x4 (&b)[4]) {
#pragma unroll
        for (int i = 0; i < 4; ++i) {
            int s = 8 * c + 2 * i + kh;
            s = s < ns ? s : ns - 1;
            const char* sb = bbase + (size_t)s * 1024;
            asm volatile("global_load_dwordx4 %0, %1, %2" : "=v"(b[i]) : "v"(boff), "s"(sb) : "memory");
        }
    };
    auto compute = [&](int c, unsigned buf, u32x4 (&b)[4], bool last) {
        const char* wb = lds + buf;
#pragma unroll
        for (int i = 0; i < 4; ++i) {
            if (!last || 8 * c + 2 * i + kh < ns) {
#pragma unroll
                for (int t = 0; t < 4; ++t) {
                    const unsigned a = trd + i * 8192 + t * 512;
                    acc[t] = mfma(tr_frag(wb, a, a + 2048), __builtin_bit_cast(bf16x8, b[i]), acc[t]);
                }
            }
        }
    };
    stage_w(0, 0);
    load_b(0, bf[0]);
    stage_w(1, K2_STG);
    load_b(1, bf[1]);
#pragma unroll
    for (int c = 0; c < 11; ++c) {
        u32x4(&b)[4] = bf[c % 3];
        if (c < 10) asm volatile("s_waitcnt vmcnt(8)" : "+v"(b[0]), "+v"(b[1]), "+v"(b[2]), "+v"(b[3])::"memory");
        else asm volatile("s_waitcnt vmcnt(0)" : "+v"(b[0]), "+v"(b[1]), "+v"(b[2]), "+v"(b[3])::"memory");
        BARRIER();
        if (c + 2 < 11) {
            stage_w(c + 2, ((c + 2) % 3) * K2_STG);
            load_b(c + 2, bf[(c + 2) % 3]);
        }
        compute(c, (c % 3) * K2_STG, b, c == 10);
    }
    BARRIER();
#pragma unroll
    for (int t = 0; t < 4; ++t)
#pragma unroll
        for (int qd = 0; qd < 4; ++qd) {
            f32x4 v = {acc[t][4 * qd], acc[t][4 * qd + 1], acc[t][4 * qd + 2], acc[t][4 * qd + 3]};
            *(f32x4*)(lds + wave * 16384 + (t * 4 + qd) * 1024 + lane * 16) = v;
        }
    BARRIER();
    const __amdgpu_buffer_rsrc_t prs = __builtin_amdgcn_make_buffer_rsrc(part, 0, 64 * 4 * 65536, 0x00020000);
#pragma unroll
    for (int i = 0; i < 6; ++i) {
        const int pc = wave * 6 + i;
        int gg = pc >> 4;
        gg = gg + (gg >= sp ? 1 : 0);
        const int pi = pc & 15;
        f32x4 a = *(const f32x4*)(lds + gg * 16384 + pi * 1024 + lane * 16);
        f32x4 b = *(const f32x4*)(lds + (gg + 4) * 16384 + pi * 1024 + lane * 16);
        a += b;
        __builtin_amdgcn_raw_buffer_store_b128(__builtin_bit_cast(u32x4, a), prs, ((tile * 4 + sp) * 4 + gg) * 16384 + pi * 1024 + lane * 16, 0, 16);
    }
    WAITV(0);
    BARRIER();
    if (tid == 0) {
        __hip_atomic_fetch_add((GAS unsigned*)(cnt + tile * 16), 1u, __ATOMIC_RELAXED, __HIP_MEMORY_SCOPE_AGENT);
        unsigned spins = 0;
        while (__hip_atomic_load((GAS unsigned*)(cnt + tile * 16), __ATOMIC_RELAXED, __HIP_MEMORY_SCOPE_AGENT) < 4u) {
            __builtin_amdgcn_s_sleep(1);
            if (++spins > (1u << 22)) break;
        }
    }
    BARRIER();
    {
        const int t = wave >> 1, q0 = 2 * (wave & 1);
        float sv[8], gv[8];
#pragma unroll
        for (int e = 0; e < 8; ++e) sv[e] = gv[e] = 0.f;
#pragma unroll
        for (int src = 0; src < 4; ++src) {
#pragma unroll
            for (int qd = 0; qd < 2; ++qd) {
                const int pi = t * 4 + q0 + qd;
                f32x4 v;
                if (src == sp) {
                    v = *(const f32x4*)(lds + sp * 16384 + pi * 1024 + lane * 16);
                    v += *(const f32x4*)(lds + (sp + 4) * 16384 + pi * 1024 + lane * 16);
                } else {
                    v = __builtin_bit_cast(f32x4, __builtin_amdgcn_raw_buffer_load_b128(prs, ((tile * 4 + src) * 4 + sp) * 16384 + pi * 1024 + lane * 16, 0, 16));
                }
#pragma unroll
                for (int e = 0; e < 4; ++e) {
                    if (src < 3) sv[qd * 4 + e] += v[e];
                    else gv[qd * 4 + e] = v[e];
                }
            }
        }
        const int row = tq * 128 + sp * 32 + r;
#pragma unroll
        for (int qd = 0; qd < 2; ++qd) {
            const int n = tn * 128 + t * 32 + 8 * (q0 + qd) + 4 * hh;
            const u32x2 xv = *(const u32x2*)((const char*)x + ((size_t)row * DM + n) * 2);
            float xf[4] = {__uint_as_float(xv[0] << 16), __uint_as_float(xv[0] & 0xffff0000u), __uint_as_float(xv[1] << 16), __uint_as_float(xv[1] & 0xffff0000u)};
            float ov[4];
#pragma unroll
            for (int e = 0; e < 4; ++e) {
                const float s = bfr(sv[qd * 4 + e]);
                const float gg = bfr(gv[qd * 4 + e]);
                const float sg = bfr(gg * __builtin_amdgcn_rcpf(1.f + __builtin_amdgcn_exp2f(-gg * 1.4426950408889634f)));
                ov[e] = xf[e] + bfr(s * sg);
            }
            u32x2 w = {cvtpk(ov[0], ov[1]), cvtpk(ov[2], ov[3])};
            *(u32x2*)((char*)out + ((size_t)row * DM + n) * 2) = w;
        }
    }
}

extern "C" void kernel_launch(void* const* d_in, const int* in_sizes, int n_in, void* d_out, int out_size, void* d_ws, size_t ws_size,
                              hipStream_t stream) {
    const __hip_bfloat16* x = (const __hip_bfloat16*)d_in[0];
    const __hip_bfloat16* kc = (const __hip_bfloat16*)d_in[1];
    const __hip_bfloat16* vc = (const __hip_bfloat16*)d_in[2];
    const __hip_bfloat16* wq = (const __hip_bfloat16*)d_in[3];
    const __hip_bfloat16* wo = (const __hip_bfloat16*)d_in[4];
    const __hip_bfloat16* wg = (const __hip_bfloat16*)d_in[6];
    const __hip_bfloat16* wu = (const __hip_bfloat16*)d_in[7];
    const __hip_bfloat16* wd = (const __hip_bfloat16*)d_in[8];
    char* ws = (char*)d_ws;
    u32x4* ofrag = (u32x4*)ws;
    u32x4* hfrag = (u32x4*)(ws + (16u << 20));
    float* part = (float*)(ws + (24u << 20));
    unsigned* cnt = (unsigned*)(ws + (40u << 20));
    hipFuncSetAttribute((const void*)k1_attn, hipFuncAttributeMaxDynamicSharedMemorySize, K1_LDS);
    hipFuncSetAttribute((const void*)k2_out, hipFuncAttributeMaxDynamicSharedMemorySize, K2_LDS);
    k1_attn<<<256, 512, K1_LDS, stream>>>(x, kc, vc, wq, wg, wu, ofrag, hfrag, cnt);
    k2_out<<<256, 512, K2_LDS, stream>>>(x, wo, wd, ofrag, hfrag, part, cnt, (__hip_bfloat16*)d_out);
}
```

```cpp
#include <hip/hip_runtime.h>
#include <hip/hip_bf16.h>
#include <stdint.h>

typedef __bf16 bf16x8 __attribute__((ext_vector_type(8)));
typedef __bf16 bf16x2 __attribute__((ext_vector_type(2)));
typedef short s16x4 __attribute__((ext_vector_type(4)));
typedef short s16x8 __attribute__((ext_vector_type(8)));
typedef float f32x16 __attribute__((ext_vector_type(16)));
typedef float f32x4 __attribute__((ext_vector_type(4)));
typedef float f32x2 __attribute__((ext_vector_type(2)));
typedef unsigned u32x4 __attribute__((ext_vector_type(4)));
typedef unsigned u32x2 __attribute__((ext_vector_type(2)));
#define LAS __attribute__((address_space(3)))
#define GAS __attribute__((address_space(1)))
#define DI __device__ __forceinline__

constexpr int SEQ = 2048, DM = 512, NH = 32, DH = 128, WIN = 512, KOFF = SEQ - WIN, FF = 1376;
constexpr int NFT = FF / 16;

DI void glds16(const void* g, unsigned ldsaddr) {
    asm volatile("s_mov_b32 m0, %0\n\ts_nop 0\n\tglobal_load_lds_dwordx4 %1, off" ::"s"(ldsaddr), "v"(g));
}
DI void glds16s(const void* sbase, unsigned voff, unsigned ldsaddr) {
    asm volatile("s_mov_b32 m0, %0\n\ts_nop 0\n\tglobal_load_lds_dwordx4 %1, %2" ::"s"(ldsaddr), "v"(voff), "s"(sbase));
}
#define WAITV(n) asm volatile("s_waitcnt vmcnt(" #n ")" ::: "memory")
#define WAITL0() asm volatile("s_waitcnt lgkmcnt(0)" ::: "memory")
#define SB0() __builtin_amdgcn_sched_barrier(0)
#define BARRIER() do { SB0(); WAITL0(); __builtin_amdgcn_s_barrier(); asm volatile("" ::: "memory"); SB0(); } while (0)
DI unsigned lane_id_opaque() {
    unsigned l;
    asm volatile("v_mbcnt_lo_u32_b32 %0, -1, 0\n\tv_mbcnt_hi_u32_b32 %0, -1, %0" : "=v"(l));
    return l;
}

DI bf16x8 tr_frag(const char* lds, unsigned a0, unsigned a1) {
    s16x4 lo = __builtin_amdgcn_ds_read_tr16_b64_v4i16((LAS s16x4*)(lds + a0));
    s16x4 hi = __builtin_amdgcn_ds_read_tr16_b64_v4i16((LAS s16x4*)(lds + a1));
    s16x8 v = __builtin_shufflevector(lo, hi, 0, 1, 2, 3, 4, 5, 6, 7);
    return __builtin_bit_cast(bf16x8, v);
}
DI bf16x8 row_frag(const char* lds, unsigned a) { return *(const LAS bf16x8*)(lds + a); }
DI f32x16 mfma(bf16x8 a, bf16x8 b, f32x16 c) { return __builtin_amdgcn_mfma_f32_32x32x16_bf16(a, b, c, 0, 0, 0); }
DI unsigned cvtpk(float lo, float hi) {
    f32x2 v = {lo, hi};
    bf16x2 b = __builtin_convertvector(v, bf16x2);
    return __builtin_bit_cast(unsigned, b);
}
DI float bfr(float x) {
    return __uint_as_float(cvtpk(x, x) << 16);
}
DI float dot2(unsigned a, unsigned b, float c) {
    return __builtin_amdgcn_fdot2_f32_bf16(__builtin_bit_cast(bf16x2, a), __builtin_bit_cast(bf16x2, b), c, false);
}
DI float sum32(float x) {
    auto rr = __builtin_amdgcn_permlane32_swap(__float_as_uint(x), __float_as_uint(x), false, false);
    return __uint_as_float(rr[0]) + __uint_as_float(rr[1]);
}
DI float max32(float x) {
    auto rr = __builtin_amdgcn_permlane32_swap(__float_as_uint(x), __float_as_uint(x), false, false);
    return fmaxf(__uint_as_float(rr[0]), __uint_as_float(rr[1]));
}
DI float max3(float a, float b, float c) { return __builtin_fmaxf(__builtin_fmaxf(a, b), c); }
DI bf16x8 pack8(const f32x16& x, int s) {
    u32x4 p = {cvtpk(x[8 * s + 0], x[8 * s + 1]), cvtpk(x[8 * s + 2], x[8 * s + 3]), cvtpk(x[8 * s + 4], x[8 * s + 5]), cvtpk(x[8 * s + 6], x[8 * s + 7])};
    return __builtin_bit_cast(bf16x8, p);
}

#define STAMP(k, i)
#define RSTAMP(k, i)
#define STAMP_DECL
#define STAMP_FLUSH(k, n)

__global__ __launch_bounds__(256) void k0_ln(const __hip_bfloat16* __restrict__ x, u32x4* __restrict__ xfrag, unsigned* __restrict__ cnt) {
    const int tid = threadIdx.x, lane = tid & 63, i = lane & 31;
    const int row = blockIdx.x * 8 + (tid >> 5);
    if (blockIdx.x == 0 && tid < 64) cnt[tid * 16] = 0u;
    const u32x4* src = (const u32x4*)((const char*)x + (size_t)row * (DM * 2) + i * 32);
    const u32x4 a = src[0], b = src[1];
    float v[16];
#pragma unroll
    for (int e = 0; e < 4; ++e) {
        v[2 * e] = __uint_as_float(a[e] << 16); v[2 * e + 1] = __uint_as_float(a[e] & 0xffff0000u);
        v[8 + 2 * e] = __uint_as_float(b[e] << 16); v[8 + 2 * e + 1] = __uint_as_float(b[e] & 0xffff0000u);
    }
    float s = 0.f;
#pragma unroll
    for (int e = 0; e < 16; ++e) s += v[e];
#pragma unroll
    for (int o = 1; o < 32; o <<= 1) s += __shfl_xor(s, o);
    const float mean = s * (1.f / DM);
    float q = 0.f;
#pragma unroll
    for (int e = 0; e < 16; ++e) { const float d = v[e] - mean; q = fmaf(d, d, q); }
#pragma unroll
    for (int o = 1; o < 32; o <<= 1) q += __shfl_xor(q, o);
    const float mb = bfr(mean);
    const float den = bfr(__builtin_sqrtf(bfr(bfr(q * (1.f / DM)) + 1e-5f)));
    u32x4 o0, o1;
#pragma unroll
    for (int e = 0; e < 4; ++e) {
        o0[e] = cvtpk(bfr(v[2 * e] - mb) / den, bfr(v[2 * e + 1] - mb) / den);
        o1[e] = cvtpk(bfr(v[8 + 2 * e] - mb) / den, bfr(v[8 + 2 * e + 1] - mb) / den);
    }
    u32x4* dst = xfrag + ((size_t)(row >> 5) * 32 + i) * 64 + (row & 31);
    dst[0] = o0;
    dst[32] = o1;
    (void)lane;
}

constexpr unsigned CB = 28672;
constexpr unsigned K1_EXT = 4 * CB;
constexpr unsigned K1_LDS = 163840;
DI constexpr unsigned kv_slot(int i) {
    const int s = i % 10;
    return s < 3 ? K1_EXT + 16384u * s : 16384u * (s - 3);
}

__global__ __launch_bounds__(512, 2) void k1_attn(const u32x4* __restrict__ xfrag, const __hip_bfloat16* __restrict__ kc,
                                                  const __hip_bfloat16* __restrict__ vc, const __hip_bfloat16* __restrict__ wq,
                                                  const __hip_bfloat16* __restrict__ wg, const __hip_bfloat16* __restrict__ wu,
                                                  u32x4* __restrict__ ofrag, u32x4* __restrict__ hfrag,
                                                  const __hip_bfloat16* __restrict__ wo, const __hip_bfloat16* __restrict__ wd) {
    extern __shared__ __attribute__((aligned(16))) char lds[];
    const int tid = threadIdx.x, lane = tid & 63;
    const int wave = __builtin_amdgcn_readfirstlane(tid >> 6);
    const int r = lane & 31, hh = lane >> 5;
    const int bid = blockIdx.x, xcd = bid & 7, idx = bid >> 3;
    const int head = xcd * 4 + (idx & 3), qb = idx >> 2;
    STAMP_DECL;
    STAMP(0, 0);
    RSTAMP(0, 30);

    const unsigned ldsb = (unsigned)(size_t)(LAS char*)lds;
    const int rot = (qb + 2 * (idx & 3)) & 7, rotk = qb;
    const char* xbase = (const char*)(xfrag + (size_t)(qb * 8 + wave) * 32 * 64);
    const unsigned xoff = lane * 16;
    const char* kbase = (const char*)kc + ((size_t)head * SEQ + KOFF) * (DH * 2);
    const char* vbase = (const char*)vc + ((size_t)head * SEQ + KOFF) * (DH * 2);
    const unsigned kp0 = (lane >> 4) * (DH * 2) + (((lane & 15) ^ (lane >> 4)) * 16);
    const char* wp;
    int wp1, wp2, wp3, wst, wrot;
    {
        const int krow = 8 * (lane >> 5) + ((lane >> 2) & 7), ch = lane & 3;
        if (wave < 4) {
            const int wld = NH * DH * 2;
            wp = (const char*)wq + (size_t)krow * wld + (head * DH + wave * 32 + ch * 8) * 2;
            wp1 = 16 * wld; wp2 = 32 * wld; wp3 = 48 * wld; wst = 64 * wld; wrot = rot;
        } else if (wave < 7) {
            const int wld = FF * 2;
            int ft = head * 3 + (wave - 4);
            ft = ft < NFT ? ft : NFT - 1;
            const char* b = (ch < 2) ? (const char*)wg : (const char*)wu;
            wp = b + (size_t)krow * wld + (ft * 16 + (ch & 1) * 8) * 2;
            wp1 = 16 * wld; wp2 = 32 * wld; wp3 = 48 * wld; wst = 64 * wld; wrot = rot;
        } else {
            wp = kbase + kp0;
            wp1 = 1024 + (int)((kp0 ^ 64) - kp0);
            wp2 = 2048 + (int)((kp0 ^ 128) - kp0);
            wp3 = 3072 + (int)((kp0 ^ 192) - kp0);
            wst = 4096; wrot = 4 * rotk;
        }
    }
    const unsigned koff = kp0 ^ (((2 * wave) & 3) << 6);
    const unsigned voff = (wave * 8 + ((lane >> 2) & 7)) * (DH * 2) + ((lane >> 5) * 32 + (lane & 3) * 8) * 2;
    const unsigned wdst = wave < 7 ? wave * 4096 : 0;
    const int wmask = wave < 7 ? 7 : 31;
    auto stage_w_p = [&](int c, int p) {
        const unsigned dst = ldsb + (wave < 7 ? (c & 3) * CB + wdst : K1_EXT + c * 4096) + p * 1024;
        const char* w = wp + (size_t)(((c + wrot) & wmask) * wst);
        if (p == 0) glds16(w, dst);
        else if (p == 1) glds16(w + wp1, dst);
        else if (p == 2) glds16(w + wp2, dst);
        else glds16(w + wp3, dst);
    };
    auto stage_kv_p = [&](int i, int p) {
        const unsigned so = ldsb + kv_slot(i) + wave * 2048 + p * 1024;
        const bool isk = (i == 0) || ((i & 1) && i < 15);
        if (isk) {
            const int j = ((((i + 1) >> 1)) + rotk) & 7;
            glds16s(kbase + (size_t)j * 64 * (DH * 2) + wave * 2048 + p * 1024, koff ^ (p * 64), so);
        } else {
            const int j = (((i == 15) ? 7 : ((i - 2) >> 1)) + rotk) & 7;
            glds16s(vbase + (size_t)j * 64 * (DH * 2) + p * 128, voff, so);
        }
    };
    u32x4 Bf[3][4];
    auto load_b = [&](int c, int ks, u32x4& dst) {
        asm volatile("global_load_dwordx4 %0, %1, %2" : "=v"(dst) : "v"(xoff), "s"(xbase + (size_t)((((c + rot) & 7) * 4 + ks) * 1024)));
    };
#pragma unroll
    for (int ks = 0; ks < 4; ++ks) load_b(0, ks, Bf[0][ks]);
#pragma unroll
    for (int p = 0; p < 4; ++p) stage_w_p(0, p);
#pragma unroll
    for (int ks = 0; ks < 4; ++ks) load_b(1, ks, Bf[1][ks]);
#pragma unroll
    for (int p = 0; p < 4; ++p) stage_w_p(1, p);
#pragma unroll
    for (int p = 0; p < 4; ++p) stage_w_p(2, p);

    f32x16 acc[7];
#pragma unroll
    for (int t = 0; t < 7; ++t) acc[t] = f32x16{};
    const int q4 = (lane & 15) >> 2, p4 = lane & 3, g16 = (lane >> 4) & 1;
    const unsigned trN = hh * 512 + q4 * 64 + g16 * 32 + p4 * 8;
    const unsigned trS = hh * 512 + q4 * 64 + g16 * 32 + (((p4 & 1) << 1) | (p4 >> 1)) * 8;
    bf16x8 A[2][7];
    auto rdA = [&](bf16x8 (&D)[7], const char* b, int ks, int t) {
        const unsigned a = (t < 4 ? trS : trN) + t * 4096 + ks * 1024;
        D[t] = tr_frag(b, a, a + 256);
    };
    auto iterA = [&](auto BSC, int c, int dma, bool lastc) {
        constexpr int BS = decltype(BSC)::value;
        const char* b = lds + (c & 3) * CB;
        const char* bn = lds + ((c + 1) & 3) * CB;
        u32x4(&Bc)[4] = Bf[BS];
        asm volatile("s_waitcnt vmcnt(12)" : "+v"(Bc[0]), "+v"(Bc[1]), "+v"(Bc[2]), "+v"(Bc[3]));
#pragma unroll
        for (int ks = 0; ks < 4; ++ks) {
            if (ks == 2) {
                WAITV(12);
                __builtin_amdgcn_s_barrier();
                SB0();
            }
#pragma unroll
            for (int t = 0; t < 7; ++t) {
                if (ks < 3) rdA(A[(ks + 1) & 1], b, ks + 1, t);
                else if (!lastc) rdA(A[0], bn, 0, t);
                acc[t] = mfma(A[ks & 1][t], __builtin_bit_cast(bf16x8, Bc[ks]), acc[t]);
                if (ks < 2 && (t == 1 || t == 4)) { if (c + 2 < 8) load_b(c + 2, 2 * ks + (t == 4), Bf[(BS + 2) % 3][2 * ks + (t == 4)]); }
                if (ks >= 2 && (t == 1 || t == 4)) {
                    const int p = 2 * (ks - 2) + (t == 4);
                    if (dma == 0) stage_w_p(c + 3, p);
                    else if (dma == 1) stage_kv_p(3 + (p >> 1), p & 1);
                    else if (dma == 2) stage_kv_p(5 + (p >> 1), p & 1);
                    else if (dma == 3) stage_kv_p(7 + (p >> 1), p & 1);
                }
                SB0();
            }
        }
    };
    using std::integral_constant;
    STAMP(0, 1);
    WAITV(12);
    BARRIER();
#pragma unroll
    for (int t = 0; t < 7; ++t) rdA(A[0], lds, 0, t);
    {
    }
    auto iterA0 = [&]() {
        const char* b = lds;
        u32x4(&Bc)[4] = Bf[0];
        asm volatile("" : "+v"(Bc[0]), "+v"(Bc[1]), "+v"(Bc[2]), "+v"(Bc[3]));
#pragma unroll
        for (int ks = 0; ks < 4; ++ks) {
            if (ks == 2) { WAITV(8); __builtin_amdgcn_s_barrier(); SB0(); }
#pragma unroll
            for (int t = 0; t < 7; ++t) {
                if (ks < 3) rdA(A[(ks + 1) & 1], b, ks + 1, t);
                else rdA(A[0], lds + CB, 0, t);
                acc[t] = mfma(A[ks & 1][t], __builtin_bit_cast(bf16x8, Bc[ks]), acc[t]);
                if (ks < 2 && (t == 1 || t == 4)) load_b(2, 2 * ks + (t == 4), Bf[2][2 * ks + (t == 4)]);
                if (ks >= 2 && (t == 1 || t == 4)) stage_w_p(3, 2 * (ks - 2) + (t == 4));
                SB0();
            }
        }
    };
    iterA0();
    iterA(integral_constant<int, 1>{}, 1, 0, false);
    iterA(integral_constant<int, 2>{}, 2, 0, false);
    iterA(integral_constant<int, 0>{}, 3, 0, false);
    iterA(integral_constant<int, 1>{}, 4, 0, false);
    STAMP(0, 2);
    auto iterAT = [&](auto BSC, auto CC) {
        constexpr int BS = decltype(BSC)::value, c = decltype(CC)::value;
        const char* b = lds + (c & 3) * CB;
        const char* bn = lds + ((c + 1) & 3) * CB;
        u32x4(&Bc)[4] = Bf[BS];
        if constexpr (c < 7) asm volatile("s_waitcnt vmcnt(12)" : "+v"(Bc[0]), "+v"(Bc[1]), "+v"(Bc[2]), "+v"(Bc[3]));
        else asm volatile("s_waitcnt vmcnt(8)" : "+v"(Bc[0]), "+v"(Bc[1]), "+v"(Bc[2]), "+v"(Bc[3]));
#pragma unroll
        for (int ks = 0; ks < 4; ++ks) {
            if (ks == 2) {
                if constexpr (c == 5) WAITV(12); else if constexpr (c == 6) WAITV(8); else WAITV(4);
                __builtin_amdgcn_s_barrier();
                SB0();
            }
#pragma unroll
            for (int t = 0; t < 7; ++t) {
                if (ks < 3) rdA(A[(ks + 1) & 1], b, ks + 1, t);
                else if constexpr (c < 7) rdA(A[0], bn, 0, t);
                acc[t] = mfma(A[ks & 1][t], __builtin_bit_cast(bf16x8, Bc[ks]), acc[t]);
                if constexpr (c + 2 < 8) { if (ks < 2 && (t == 1 || t == 4)) load_b(c + 2, 2 * ks + (t == 4), Bf[(BS + 2) % 3][2 * ks + (t == 4)]); }
                if (ks >= 2 && (t == 1 || t == 4)) {
                    const int p = 2 * (ks - 2) + (t == 4);
                    stage_kv_p(2 + 2 * (c - 5) + (p >> 1), p & 1);
                }
                SB0();
            }
        }
    };
    iterAT(integral_constant<int, 2>{}, integral_constant<int, 5>{});
    iterAT(integral_constant<int, 0>{}, integral_constant<int, 6>{});
    iterAT(integral_constant<int, 1>{}, integral_constant<int, 7>{});
    STAMP(0, 3);
    WAITV(4);
    BARRIER();
    stage_kv_p(8, 0); stage_kv_p(8, 1); stage_kv_p(9, 0); stage_kv_p(9, 1);
    bf16x8 qf[8];
    {
#pragma unroll
        for (int t = 4; t < 7; ++t) {
            f32x16 hv;
#pragma unroll
            for (int e = 0; e < 8; ++e) {
                const float g = acc[t][e], u = acc[t][8 + e];
                const float z = g * (1.f + 0.044715f * g * g) * (2.f * 0.7978845608028654f * 1.4426950408889634f);
                hv[e] = g * __builtin_amdgcn_rcpf(1.f + __builtin_amdgcn_exp2f(-z)) * u;
            }
            const int ft = head * 3 + (t - 4);
            if (ft < NFT) hfrag[((size_t)(qb * 8 + wave) * NFT + ft) * 64 + lane] = __builtin_bit_cast(u32x4, pack8(hv, 0));
        }
        const float qs = 0.08838834764831845f * 1.4426950408889634f;
#pragma unroll
        for (int t = 0; t < 4; ++t) {
#pragma unroll
            for (int e = 0; e < 16; ++e) acc[t][e] *= qs;
            qf[2 * t] = pack8(acc[t], 0);
            qf[2 * t + 1] = pack8(acc[t], 1);
        }
    }
    SB0();
    STAMP(0, 4);

    f32x16 o[4];
#pragma unroll
    for (int t = 0; t < 4; ++t) o[t] = f32x16{};
    float m, l = 0.f;
    f32x16 negm;
    const unsigned krd = r * 256 + ((hh ^ (r & 15)) * 16);
    const unsigned vrd = (hh * 4 + q4) * 64 + g16 * 32 + p4 * 8;
    auto rdK = [&](int j, int i) {
        const char* kb = lds + kv_slot(j == 0 ? 0 : 2 * j - 1);
        return row_frag(kb, (krd ^ ((i >> 1) * 32)) + (i & 1) * 8192);
    };
    auto rdV = [&](int j, int i) {
        const char* vb = lds + kv_slot(j == 7 ? 15 : 2 * j + 2);
        const unsigned a = vrd + (2 * (i >> 2) * 4 + (i & 3)) * 512;
        return tr_frag(vb, a, a + 2048);
    };
#define PDV 5
    constexpr int PD = PDV;
    bf16x8 fr[PD + 1];
    f32x16 S[2][2];
    bf16x8 pf[4];
    {
#pragma unroll
        for (int i = 0; i < PD; ++i) fr[i % (PD + 1)] = rdK(0, i);
#pragma unroll
        for (int i = 0; i < 16; ++i) {
            if (i + PD < 16) fr[(i + PD) % (PD + 1)] = rdK(0, i + PD);
            else fr[(i + PD) % (PD + 1)] = rdK(1, i + PD - 16);
            S[0][i & 1] = mfma(fr[i % (PD + 1)], qf[i >> 1], i < 2 ? f32x16{} : S[0][i & 1]);
            SB0();
        }
        float pm = max3(S[0][0][0], S[0][0][1], S[0][1][0]);
#pragma unroll
        for (int e = 2; e < 16; ++e) pm = fmaxf(pm, S[0][0][e]);
#pragma unroll
        for (int e = 1; e < 16; ++e) pm = fmaxf(pm, S[0][1][e]);
        m = max32(pm);
#pragma unroll
        for (int e = 0; e < 16; ++e) { S[0][0][e] -= m; S[0][1][e] -= m; negm[e] = -m; }
    }
    auto iterB = [&](auto JC, auto DC) {
        constexpr int j = decltype(JC)::value;
        constexpr int dma0 = decltype(DC)::value;
        constexpr int c = j & 1, n = c ^ 1;
        constexpr int g0 = 16 + 32 * j;
        float ps0 = 0.f, ps1 = 0.f;
#pragma unroll
        for (int i = 0; i < 16; ++i) {
            if constexpr (j < 7) {
                if (i + PD < 16) fr[(g0 + i + PD) % (PD + 1)] = rdK(j + 1, i + PD);
                else fr[(g0 + i + PD) % (PD + 1)] = rdV(j, i + PD - 16);
                S[n][i & 1] = mfma(fr[(g0 + i) % (PD + 1)], qf[i >> 1], i < 2 ? negm : S[n][i & 1]);
            }
            S[c][0][i] = __builtin_amdgcn_exp2f(S[c][0][i]);
            if (i & 1) ps1 += S[c][0][i]; else ps0 += S[c][0][i];
            if ((i & 7) == 7) pf[i >> 3] = pack8(S[c][0], i >> 3);
            if constexpr (dma0 != 0) { if (i == 3 || i == 11) stage_kv_p(dma0, i >> 3); }
            SB0();
        }
        constexpr int g1 = (j < 7) ? g0 + 16 : g0;
        float pm = -3e38f;
#pragma unroll
        for (int i = 0; i < 16; ++i) {
            if (i + PD < 16) fr[(g1 + i + PD) % (PD + 1)] = rdV(j, i + PD);
            else if constexpr (j < 6) fr[(g1 + i + PD) % (PD + 1)] = rdK(j + 2, i + PD - 16);
            else if constexpr (j == 6) fr[(g1 + i + PD) % (PD + 1)] = rdV(7, i + PD - 16);
            if (i < 8) {
                S[c][1][2 * i] = __builtin_amdgcn_exp2f(S[c][1][2 * i]);
                S[c][1][2 * i + 1] = __builtin_amdgcn_exp2f(S[c][1][2 * i + 1]);
                ps0 += S[c][1][2 * i];
                ps1 += S[c][1][2 * i + 1];
                if (i == 3) pf[2] = pack8(S[c][1], 0);
                if (i == 7) pf[3] = pack8(S[c][1], 1);
            }
            o[i & 3] = mfma(fr[(g1 + i) % (PD + 1)], pf[i >> 2], o[i & 3]);
            if constexpr (j < 7) { if (i >= 8) { pm = max3(pm, S[n][(i - 8) >> 2][(4 * (i - 8)) & 15], S[n][(i - 8) >> 2][((4 * (i - 8)) & 15) + 1]); pm = max3(pm, S[n][(i - 8) >> 2][((4 * (i - 8)) & 15) + 2], S[n][(i - 8) >> 2][((4 * (i - 8)) & 15) + 3]); } }
            if constexpr (dma0 != 0 && dma0 + 1 <= 15) { if (i == 3 || i == 11) stage_kv_p(dma0 + 1, i >> 3); }
            SB0();
        }
        l += ps0 + ps1;
        if constexpr (j < 7) {
            pm = max32(pm);
            if (__builtin_expect(__any(pm > 8.f), 0)) {
                const float d = fmaxf(pm, 0.f);
                const float al = __builtin_amdgcn_exp2f(-d);
                m += d;
                l *= al;
#pragma unroll
                for (int e = 0; e < 16; ++e) { S[n][0][e] -= d; S[n][1][e] -= d; negm[e] = -m; }
#pragma unroll
                for (int t = 0; t < 4; ++t)
#pragma unroll
                    for (int e = 0; e < 16; ++e) o[t][e] *= al;
            }
        }
    };
#define RAWBAR() do { __builtin_amdgcn_s_barrier(); SB0(); } while (0)
    iterB(integral_constant<int, 0>{}, integral_constant<int, 0>{});
    STAMP(0, 5);
    RAWBAR();
    iterB(integral_constant<int, 1>{}, integral_constant<int, 10>{});
    STAMP(0, 6);
    WAITV(8); RAWBAR();
    iterB(integral_constant<int, 2>{}, integral_constant<int, 12>{});
    STAMP(0, 7);
    WAITV(8); RAWBAR();
    iterB(integral_constant<int, 3>{}, integral_constant<int, 14>{});
    unsigned pfd = 0;
    {
        const int sp2 = xcd & 3;
        int row = idx * 43 + wave * 8 + (lane >> 3);
        row = row < idx * 43 + 43 ? row : idx * 43 + 42;
        int grow = (sp2 < 3 ? sp2 * 1376 : 0) + row;
        grow = grow < (sp2 < 3 ? NH * DH : FF) ? grow : (sp2 < 3 ? NH * DH : FF) - 1;
        const char* pp = (const char*)(sp2 < 3 ? wo : wd) + (size_t)grow * (DM * 2) + (lane & 7) * 128;
        if (wave < 6) asm volatile("global_load_ushort %0, %1, off" : "=v"(pfd) : "v"(pp));
    }
    STAMP(0, 8);
    WAITV(8); RAWBAR();
    iterB(integral_constant<int, 4>{}, integral_constant<int, 0>{});
    STAMP(0, 9);
    WAITV(4); RAWBAR();
    iterB(integral_constant<int, 5>{}, integral_constant<int, 0>{});
    STAMP(0, 10);
    WAITV(0); RAWBAR();
    asm volatile("" ::"v"(pfd));
    iterB(integral_constant<int, 6>{}, integral_constant<int, 0>{});
    STAMP(0, 11);
    iterB(integral_constant<int, 7>{}, integral_constant<int, 0>{});
    STAMP(0, 12);

    l = sum32(l);
    const float rl = 1.f / l;
    const unsigned lane2 = lane_id_opaque();
#pragma unroll
    for (int t = 0; t < 4; ++t) {
#pragma unroll
        for (int e = 0; e < 16; ++e) o[t][e] *= rl;
        u32x4* dst = ofrag + ((size_t)(qb * 8 + wave) * 256 + head * 8 + 2 * t) * 64 + lane2;
        dst[0] = __builtin_bit_cast(u32x4, pack8(o[t], 0));
        dst[64] = __builtin_bit_cast(u32x4, pack8(o[t], 1));
    }
    STAMP(0, 13);
    RSTAMP(0, 31);
    STAMP_FLUSH(0, 14);
}

constexpr unsigned K2_STG = 32768;
constexpr unsigned K2_LDS = 4 * K2_STG + 1024;

__global__ __launch_bounds__(512, 2) void k2_out(const __hip_bfloat16* __restrict__ x, const __hip_bfloat16* __restrict__ wo,
                                                 const __hip_bfloat16* __restrict__ wd, const u32x4* __restrict__ ofrag,
                                                 const u32x4* __restrict__ hfrag, float* __restrict__ part, unsigned* __restrict__ cnt,
                                                 __hip_bfloat16* __restrict__ out) {
    extern __shared__ __attribute__((aligned(16))) char lds[];
    const int tid = threadIdx.x, lane = tid & 63;
    const int wave = __builtin_amdgcn_readfirstlane(tid >> 6);
    const int r = lane & 31, hh = lane >> 5;
    const int bid = blockIdx.x, xcd = bid & 7, idx = bid >> 3;
    const int sp = xcd & 3, tq = (xcd >> 2) * 8 + (idx >> 2), tn = idx & 3;
    const int tile = tq * 4 + tn;
    const int g = wave & 3, kh = wave >> 2;
    STAMP_DECL;
    STAMP(1, 0);
    const int s0 = sp < 3 ? sp * 86 : 0;
    const int ns = sp == 2 ? 84 : 86;
    const int ktot = sp < 3 ? NH * DH : FF;
    const char* wbase = (const char*)(sp < 3 ? wo : wd);
    const unsigned ldsb = (unsigned)(size_t)(LAS char*)lds;
    const char* bbase = (const char*)(sp < 3 ? ofrag + ((size_t)(tq * 4 + g) * 256 + s0) * 64 : hfrag + (size_t)(tq * 4 + g) * NFT * 64);
    const unsigned boff = lane * 16;
    const int wrow = s0 * 16 + wave * 16 + ((lane >> 2) & 7);
    const unsigned wcol = (tn * 128 + (lane >> 5) * 32 + (lane & 3) * 8) * 2;
    const int rot2 = 0;
    auto phys = [&](int c) { int q = c + rot2; q = q >= 10 ? q - 10 : q; return c < 10 ? q : 10; };
    auto stage_w = [&](int c, int half) {
        int r0 = wrow + phys(c) * 128 + half * 8;
        r0 = r0 < ktot ? r0 : ktot - 1;
        const unsigned a0 = r0 * (DM * 2) + wcol;
        const unsigned o_ = ldsb + (c & 3) * K2_STG + wave * 4096 + half * 2048;
        glds16s(wbase, a0, o_);
        glds16s(wbase + 128, a0, o_ + 1024);
    };
    f32x16 acc[4];
#pragma unroll
    for (int t = 0; t < 4; ++t) acc[t] = f32x16{};
    const int q4 = (lane & 15) >> 2, p4 = lane & 3, g16 = (lane >> 4) & 1;
    const unsigned trd = (hh * 4 + q4) * 64 + g16 * 32 + p4 * 8 + kh * 4096;
    u32x4 bf[4][4];
    auto load_b = [&](int c, int i) {
        int s = 8 * phys(c) + 2 * i + kh;
        s = s < ns ? s : ns - 1;
        const char* sb = bbase + (size_t)s * 1024;
        asm volatile("global_load_dwordx4 %0, %1, %2" : "=v"(bf[c & 3][i]) : "v"(boff), "s"(sb));
    };
    auto rdW = [&](int c, int i) {
        const unsigned a = trd + (i >> 2) * 8192 + (i & 3) * 512;
        return tr_frag(lds + (c & 3) * K2_STG, a, a + 2048);
    };
    constexpr int PD = 6;
    bf16x8 fr[PD + 1];
#pragma unroll
    for (int c = 0; c < 3; ++c) {
        stage_w(c, 0); stage_w(c, 1);
        load_b(c, 0); load_b(c, 1); load_b(c, 2); load_b(c, 3);
    }
    auto stage_iter = [&](auto CC) {
        constexpr int c = decltype(CC)::value;
        u32x4(&b)[4] = bf[c & 3];
        if constexpr (c < 9) asm volatile("s_waitcnt vmcnt(16)" : "+v"(b[0]), "+v"(b[1]), "+v"(b[2]), "+v"(b[3])::"memory");
        else if constexpr (c == 9) asm volatile("s_waitcnt vmcnt(8)" : "+v"(b[0]), "+v"(b[1]), "+v"(b[2]), "+v"(b[3])::"memory");
        else asm volatile("s_waitcnt vmcnt(0)" : "+v"(b[0]), "+v"(b[1]), "+v"(b[2]), "+v"(b[3])::"memory");
        BARRIER();
        STAMP(1, 1 + c);
#pragma unroll
        for (int i = 0; i < PD; ++i) fr[i % (PD + 1)] = rdW(c, i);
#pragma unroll
        for (int i = 0; i < 16; ++i) {
            if (i + PD < 16) fr[(i + PD) % (PD + 1)] = rdW(c, i + PD);
            if (c < 10 || 8 * c + 2 * (i >> 2) + kh < ns) acc[i & 3] = mfma(fr[i % (PD + 1)], __builtin_bit_cast(bf16x8, b[i >> 2]), acc[i & 3]);
            if constexpr (c + 3 < 11) {
                if (i == 0) stage_w(c + 3, 0);
                if (i == 2) stage_w(c + 3, 1);
                if (i == 4) load_b(c + 3, 0);
                if (i == 6) load_b(c + 3, 1);
                if (i == 8) load_b(c + 3, 2);
                if (i == 10) load_b(c + 3, 3);
            }
            SB0();
        }
    };
    using std::integral_constant;
    stage_iter(integral_constant<int, 0>{});
    stage_iter(integral_constant<int, 1>{});
    stage_iter(integral_constant<int, 2>{});
    stage_iter(integral_constant<int, 3>{});
    stage_iter(integral_constant<int, 4>{});
    stage_iter(integral_constant<int, 5>{});
    stage_iter(integral_constant<int, 6>{});
    stage_iter(integral_constant<int, 7>{});
    stage_iter(integral_constant<int, 8>{});
    stage_iter(integral_constant<int, 9>{});
    stage_iter(integral_constant<int, 10>{});
    BARRIER();
    STAMP(1, 12);
#pragma unroll
    for (int t = 0; t < 4; ++t)
#pragma unroll
        for (int qd = 0; qd < 4; ++qd) {
            f32x4 v = {acc[t][4 * qd], acc[t][4 * qd + 1], acc[t][4 * qd + 2], acc[t][4 * qd + 3]};
            *(f32x4*)(lds + wave * 16384 + (t * 4 + qd) * 1024 + lane * 16) = v;
        }
    BARRIER();
    const __amdgpu_buffer_rsrc_t prs = __builtin_amdgcn_make_buffer_rsrc(part, 0, 64 * 4 * 65536, 0x00020000);
#pragma unroll
    for (int i = 0; i < 6; ++i) {
        const int pc = wave * 6 + i;
        int gg = pc >> 4;
        gg = gg + (gg >= sp ? 1 : 0);
        const int pi = pc & 15;
        f32x4 a = *(const f32x4*)(lds + gg * 16384 + pi * 1024 + lane * 16);
        f32x4 b = *(const f32x4*)(lds + (gg + 4) * 16384 + pi * 1024 + lane * 16);
        a += b;
        __builtin_amdgcn_raw_buffer_store_b128(__builtin_bit_cast(u32x4, a), prs, ((tile * 4 + sp) * 4 + gg) * 16384 + pi * 1024 + lane * 16, 0, 16);
    }
    STAMP(1, 13);
    WAITV(0);
    BARRIER();
    STAMP(1, 14);
    if (tid == 0) {
        __hip_atomic_fetch_add((GAS unsigned*)(cnt + tile * 16), 1u, __ATOMIC_RELAXED, __HIP_MEMORY_SCOPE_AGENT);
        unsigned spins = 0;
        while (__hip_atomic_load((GAS unsigned*)(cnt + tile * 16), __ATOMIC_RELAXED, __HIP_MEMORY_SCOPE_AGENT) < 4u) {
            __builtin_amdgcn_s_sleep(1);
            if (++spins > (1u << 22)) break;
        }
    }
    BARRIER();
    STAMP(1, 15);
    {
        const int t = wave >> 1, q0 = 2 * (wave & 1);
        float sv[8], gv[8];
#pragma unroll
        for (int e = 0; e < 8; ++e) sv[e] = gv[e] = 0.f;
#pragma unroll
        for (int src = 0; src < 4; ++src) {
#pragma unroll
            for (int qd = 0; qd < 2; ++qd) {
                const int pi = t * 4 + q0 + qd;
                f32x4 v;
                if (src == sp) {
                    v = *(const f32x4*)(lds + sp * 16384 + pi * 1024 + lane * 16);
                    v += *(const f32x4*)(lds + (sp + 4) * 16384 + pi * 1024 + lane * 16);
                } else {
                    v = __builtin_bit_cast(f32x4, __builtin_amdgcn_raw_buffer_load_b128(prs, ((tile * 4 + src) * 4 + sp) * 16384 + pi * 1024 + lane * 16, 0, 16));
                }
#pragma unroll
                for (int e = 0; e < 4; ++e) {
                    if (src < 3) sv[qd * 4 + e] += v[e];
                    else gv[qd * 4 + e] = v[e];
                }
            }
        }
        const int row = tq * 128 + sp * 32 + r;
#pragma unroll
        for (int qd = 0; qd < 2; ++qd) {
            const int n = tn * 128 + t * 32 + 8 * (q0 + qd) + 4 * hh;
            const u32x2 xv = *(const u32x2*)((const char*)x + ((size_t)row * DM + n) * 2);
            float xf[4] = {__uint_as_float(xv[0] << 16), __uint_as_float(xv[0] & 0xffff0000u), __uint_as_float(xv[1] << 16), __uint_as_float(xv[1] & 0xffff0000u)};
            float ov[4];
#pragma unroll
            for (int e = 0; e < 4; ++e) {
                const float s = bfr(sv[qd * 4 + e]);
                const float gg = bfr(gv[qd * 4 + e]);
                const float sg = bfr(gg * __builtin_amdgcn_rcpf(1.f + __builtin_amdgcn_exp2f(-gg * 1.4426950408889634f)));
                ov[e] = xf[e] + bfr(s * sg);
            }
            u32x2 w = {cvtpk(ov[0], ov[1]), cvtpk(ov[2], ov[3])};
            *(u32x2*)((char*)out + ((size_t)row * DM + n) * 2) = w;
        }
    }
    STAMP(1, 16);
    STAMP(1, 17);
    STAMP_FLUSH(1, 18);
}

extern "C" void kernel_launch(void* const* d_in, const int* in_sizes, int n_in, void* d_out, int out_size, void* d_ws, size_t ws_size,
                              hipStream_t stream) {
    const __hip_bfloat16* x = (const __hip_bfloat16*)d_in[0];
    const __hip_bfloat16* kc = (const __hip_bfloat16*)d_in[1];
    const __hip_bfloat16* vc = (const __hip_bfloat16*)d_in[2];
    const __hip_bfloat16* wq = (const __hip_bfloat16*)d_in[3];
    const __hip_bfloat16* wo = (const __hip_bfloat16*)d_in[4];
    const __hip_bfloat16* wg = (const __hip_bfloat16*)d_in[6];
    const __hip_bfloat16* wu = (const __hip_bfloat16*)d_in[7];
    const __hip_bfloat16* wd = (const __hip_bfloat16*)d_in[8];
    char* ws = (char*)d_ws;
    u32x4* ofrag = (u32x4*)ws;
    u32x4* hfrag = (u32x4*)(ws + (16u << 20));
    float* part = (float*)(ws + (24u << 20));
    unsigned* cnt = (unsigned*)(ws + (40u << 20));
    u32x4* xfrag = (u32x4*)(ws + (41u << 20));
    (void)hipFuncSetAttribute((const void*)k1_attn, hipFuncAttributeMaxDynamicSharedMemorySize, K1_LDS);
    (void)hipFuncSetAttribute((const void*)k2_out, hipFuncAttributeMaxDynamicSharedMemorySize, K2_LDS);
    k0_ln<<<256, 256, 0, stream>>>(x, xfrag, cnt);
    k1_attn<<<256, 512, K1_LDS, stream>>>(xfrag, kc, vc, wq, wg, wu, ofrag, hfrag, wo, wd);
    k2_out<<<256, 512, K2_LDS, stream>>>(x, wo, wd, ofrag, hfrag, part, cnt, (__hip_bfloat16*)d_out);
}
```

```cpp
#include <hip/hip_runtime.h>
#include <hip/hip_bf16.h>
#include <stdint.h>

typedef __bf16 bf16x8 __attribute__((ext_vector_type(8)));
typedef __bf16 bf16x2 __attribute__((ext_vector_type(2)));
typedef short s16x4 __attribute__((ext_vector_type(4)));
typedef short s16x8 __attribute__((ext_vector_type(8)));
typedef float f32x16 __attribute__((ext_vector_type(16)));
typedef float f32x4 __attribute__((ext_vector_type(4)));
typedef float f32x2 __attribute__((ext_vector_type(2)));
typedef unsigned u32x4 __attribute__((ext_vector_type(4)));
typedef unsigned u32x2 __attribute__((ext_vector_type(2)));
#define LAS __attribute__((address_space(3)))
#define GAS __attribute__((address_space(1)))
#define DI __device__ __forceinline__

constexpr int SEQ = 2048, DM = 512, NH = 32, DH = 128, WIN = 512, KOFF = SEQ - WIN, FF = 1376;
constexpr int NFT = FF / 16;

DI void glds16(const void* g, unsigned ldsaddr) {
    asm volatile("s_mov_b32 m0, %0\n\ts_nop 0\n\tglobal_load_lds_dwordx4 %1, off" ::"s"(ldsaddr), "v"(g));
}
DI void glds16s(const void* sbase, unsigned voff, unsigned ldsaddr) {
    asm volatile("s_mov_b32 m0, %0\n\ts_nop 0\n\tglobal_load_lds_dwordx4 %1, %2" ::"s"(ldsaddr), "v"(voff), "s"(sbase));
}
#define WAITV(n) asm volatile("s_waitcnt vmcnt(" #n ")" ::: "memory")
#define WAITL0() asm volatile("s_waitcnt lgkmcnt(0)" ::: "memory")
#define SB0() __builtin_amdgcn_sched_barrier(0)
#define BARRIER() do { SB0(); WAITL0(); __builtin_amdgcn_s_barrier(); asm volatile("" ::: "memory"); SB0(); } while (0)
DI unsigned lane_id_opaque() {
    unsigned l;
    asm volatile("v_mbcnt_lo_u32_b32 %0, -1, 0\n\tv_mbcnt_hi_u32_b32 %0, -1, %0" : "=v"(l));
    return l;
}

DI bf16x8 tr_frag(const char* lds, unsigned a0, unsigned a1) {
    s16x4 lo = __builtin_amdgcn_ds_read_tr16_b64_v4i16((LAS s16x4*)(lds + a0));
    s16x4 hi = __builtin_amdgcn_ds_read_tr16_b64_v4i16((LAS s16x4*)(lds + a1));
    s16x8 v = __builtin_shufflevector(lo, hi, 0, 1, 2, 3, 4, 5, 6, 7);
    return __builtin_bit_cast(bf16x8, v);
}
DI bf16x8 row_frag(const char* lds, unsigned a) { return *(const LAS bf16x8*)(lds + a); }
DI f32x16 mfma(bf16x8 a, bf16x8 b, f32x16 c) { return __builtin_amdgcn_mfma_f32_32x32x16_bf16(a, b, c, 0, 0, 0); }
DI unsigned cvtpk(float lo, float hi) {
    f32x2 v = {lo, hi};
    bf16x2 b = __builtin_convertvector(v, bf16x2);
    return __builtin_bit_cast(unsigned, b);
}
DI float bfr(float x) {
    return __uint_as_float(cvtpk(x, x) << 16);
}
DI float dot2(unsigned a, unsigned b, float c) {
    return __builtin_amdgcn_fdot2_f32_bf16(__builtin_bit_cast(bf16x2, a), __builtin_bit_cast(bf16x2, b), c, false);
}
DI float sum32(float x) {
    auto rr = __builtin_amdgcn_permlane32_swap(__float_as_uint(x), __float_as_uint(x), false, false);
    return __uint_as_float(rr[0]) + __uint_as_float(rr[1]);
}
DI float max32(float x) {
    auto rr = __builtin_amdgcn_permlane32_swap(__float_as_uint(x), __float_as_uint(x), false, false);
    return fmaxf(__uint_as_float(rr[0]), __uint_as_float(rr[1]));
}
DI float max3(float a, float b, float c) { return __builtin_fmaxf(__builtin_fmaxf(a, b), c); }
DI bf16x8 pack8(const f32x16& x, int s) {
    u32x4 p = {cvtpk(x[8 * s + 0], x[8 * s + 1]), cvtpk(x[8 * s + 2], x[8 * s + 3]), cvtpk(x[8 * s + 4], x[8 * s + 5]), cvtpk(x[8 * s + 6], x[8 * s + 7])};
    return __builtin_bit_cast(bf16x8, p);
}

#define STAMP(k, i)
#define RSTAMP(k, i)
#define STAMP_DECL
#define STAMP_FLUSH(k, n)

__global__ __launch_bounds__(256) void k0_ln(const __hip_bfloat16* __restrict__ x, u32x4* __restrict__ xfrag, unsigned* __restrict__ cnt,
                                             const __hip_bfloat16* __restrict__ wq, const __hip_bfloat16* __restrict__ wg,
                                             const __hip_bfloat16* __restrict__ wu) {
    const int tid = threadIdx.x, lane = tid & 63, i = lane & 31;
    const int row = blockIdx.x * 8 + (tid >> 5);
    if (blockIdx.x == 0 && tid < 64) cnt[tid * 16] = 0u;
    const u32x4* src = (const u32x4*)((const char*)x + (size_t)row * (DM * 2) + i * 32);
    const u32x4 a = src[0], b = src[1];
    unsigned short pf = 0;
    {
        const int xcd = blockIdx.x & 7, idx = blockIdx.x >> 3, wave = tid >> 6;
        const char* pp;
        if (wave < 2) pp = (const char*)wq + (size_t)(idx * 16 + wave * 8 + (lane >> 3)) * (NH * DH * 2) + xcd * 1024 + (lane & 7) * 128;
        else {
            int cb = xcd * 384 + (lane & 3) * 128;
            cb = cb < FF * 2 - 2 ? cb : FF * 2 - 2;
            pp = (const char*)(wave == 2 ? wg : wu) + (size_t)(idx * 16 + (lane >> 2)) * (FF * 2) + cb;
        }
        pf = *(const volatile unsigned short*)pp;
    }
    float v[16];
#pragma unroll
    for (int e = 0; e < 4; ++e) {
        v[2 * e] = __uint_as_float(a[e] << 16); v[2 * e + 1] = __uint_as_float(a[e] & 0xffff0000u);
        v[8 + 2 * e] = __uint_as_float(b[e] << 16); v[8 + 2 * e + 1] = __uint_as_float(b[e] & 0xffff0000u);
    }
    float s = 0.f;
#pragma unroll
    for (int e = 0; e < 16; ++e) s += v[e];
#pragma unroll
    for (int o = 1; o < 32; o <<= 1) s += __shfl_xor(s, o);
    const float mean = s * (1.f / DM);
    float q = 0.f;
#pragma unroll
    for (int e = 0; e < 16; ++e) { const float d = v[e] - mean; q = fmaf(d, d, q); }
#pragma unroll
    for (int o = 1; o < 32; o <<= 1) q += __shfl_xor(q, o);
    const float mb = bfr(mean);
    const float den = bfr(__builtin_sqrtf(bfr(bfr(q * (1.f / DM)) + 1e-5f)));
    u32x4 o0, o1;
#pragma unroll
    for (int e = 0; e < 4; ++e) {
        o0[e] = cvtpk(bfr(v[2 * e] - mb) / den, bfr(v[2 * e + 1] - mb) / den);
        o1[e] = cvtpk(bfr(v[8 + 2 * e] - mb) / den, bfr(v[8 + 2 * e + 1] - mb) / den);
    }
    u32x4* dst = xfrag + ((size_t)(row >> 5) * 32 + i) * 64 + (row & 31);
    dst[0] = o0;
    dst[32] = o1;
    asm volatile("" ::"v"((unsigned)pf));
}

constexpr unsigned CB = 28672;
constexpr unsigned K1_EXT = 4 * CB;
constexpr unsigned K1_LDS = 163840;
DI constexpr unsigned kv_slot(int i) {
    const int s = i % 10;
    return s < 3 ? K1_EXT + 16384u * s : 16384u * (s - 3);
}

__global__ __launch_bounds__(512, 2) void k1_attn(const u32x4* __restrict__ xfrag, const __hip_bfloat16* __restrict__ kc,
                                                  const __hip_bfloat16* __restrict__ vc, const __hip_bfloat16* __restrict__ wq,
                                                  const __hip_bfloat16* __restrict__ wg, const __hip_bfloat16* __restrict__ wu,
                                                  u32x4* __restrict__ ofrag, u32x4* __restrict__ hfrag,
                                                  const __hip_bfloat16* __restrict__ wo, const __hip_bfloat16* __restrict__ wd) {
    extern __shared__ __attribute__((aligned(16))) char lds[];
    const int tid = threadIdx.x, lane = tid & 63;
    const int wave = __builtin_amdgcn_readfirstlane(tid >> 6);
    const int r = lane & 31, hh = lane >> 5;
    const int bid = blockIdx.x, xcd = bid & 7, idx = bid >> 3;
    const int head = xcd * 4 + (idx & 3), qb = idx >> 2;
    STAMP_DECL;
    STAMP(0, 0);
    RSTAMP(0, 30);

    const unsigned ldsb = (unsigned)(size_t)(LAS char*)lds;
    const int rot = 0, rotk = 0;
    const char* xbase = (const char*)(xfrag + (size_t)(qb * 8 + wave) * 32 * 64);
    const unsigned xoff = lane * 16;
    const char* kbase = (const char*)kc + ((size_t)head * SEQ + KOFF) * (DH * 2);
    const char* vbase = (const char*)vc + ((size_t)head * SEQ + KOFF) * (DH * 2);
    const unsigned kp0 = (lane >> 4) * (DH * 2) + (((lane & 15) ^ (lane >> 4)) * 16);
    const char* wp;
    int wp1, wp2, wp3, wst, wrot;
    {
        const int krow = 8 * (lane >> 5) + ((lane >> 2) & 7), ch = lane & 3;
        if (wave < 4) {
            const int wld = NH * DH * 2;
            wp = (const char*)wq + (size_t)krow * wld + (head * DH + wave * 32 + ch * 8) * 2;
            wp1 = 16 * wld; wp2 = 32 * wld; wp3 = 48 * wld; wst = 64 * wld; wrot = rot;
        } else if (wave < 7) {
            const int wld = FF * 2;
            int ft = head * 3 + (wave - 4);
            ft = ft < NFT ? ft : NFT - 1;
            const char* b = (ch < 2) ? (const char*)wg : (const char*)wu;
            wp = b + (size_t)krow * wld + (ft * 16 + (ch & 1) * 8) * 2;
            wp1 = 16 * wld; wp2 = 32 * wld; wp3 = 48 * wld; wst = 64 * wld; wrot = rot;
        } else {
            wp = kbase + kp0;
            wp1 = 1024 + (int)((kp0 ^ 64) - kp0);
            wp2 = 2048 + (int)((kp0 ^ 128) - kp0);
            wp3 = 3072 + (int)((kp0 ^ 192) - kp0);
            wst = 4096; wrot = 4 * rotk;
        }
    }
    const unsigned koff = kp0 ^ (((2 * wave) & 3) << 6);
    const unsigned voff = (wave * 8 + ((lane >> 2) & 7)) * (DH * 2) + ((lane >> 5) * 32 + (lane & 3) * 8) * 2;
    const unsigned wdst = wave < 7 ? wave * 4096 : 0;
    const int wmask = wave < 7 ? 7 : 31;
    auto stage_w_p = [&](int c, int p) {
        const unsigned dst = ldsb + (wave < 7 ? (c & 3) * CB + wdst : K1_EXT + c * 4096) + p * 1024;
        const char* w = wp + (size_t)(((c + wrot) & wmask) * wst);
        if (p == 0) glds16(w, dst);
        else if (p == 1) glds16(w + wp1, dst);
        else if (p == 2) glds16(w + wp2, dst);
        else glds16(w + wp3, dst);
    };
    auto stage_kv_p = [&](int i, int p) {
        const unsigned so = ldsb + kv_slot(i) + wave * 2048 + p * 1024;
        const bool isk = (i == 0) || ((i & 1) && i < 15);
        if (isk) {
            const int j = ((((i + 1) >> 1)) + rotk) & 7;
            glds16s(kbase + (size_t)j * 64 * (DH * 2) + wave * 2048 + p * 1024, koff ^ (p * 64), so);
        } else {
            const int j = (((i == 15) ? 7 : ((i - 2) >> 1)) + rotk) & 7;
            glds16s(vbase + (size_t)j * 64 * (DH * 2) + p * 128, voff, so);
        }
    };
    u32x4 Bf[3][4];
    auto load_b = [&](int c, int ks, u32x4& dst) {
        asm volatile("global_load_dwordx4 %0, %1, %2" : "=v"(dst) : "v"(xoff), "s"(xbase + (size_t)((((c + rot) & 7) * 4 + ks) * 1024)));
    };
#pragma unroll
    for (int ks = 0; ks < 4; ++ks) load_b(0, ks, Bf[0][ks]);
#pragma unroll
    for (int p = 0; p < 4; ++p) stage_w_p(0, p);
#pragma unroll
    for (int ks = 0; ks < 4; ++ks) load_b(1, ks, Bf[1][ks]);
#pragma unroll
    for (int p = 0; p < 4; ++p) stage_w_p(1, p);
#pragma unroll
    for (int p = 0; p < 4; ++p) stage_w_p(2, p);

    f32x16 acc[7];
#pragma unroll
    for (int t = 0; t < 7; ++t) acc[t] = f32x16{};
    const int q4 = (lane & 15) >> 2, p4 = lane & 3, g16 = (lane >> 4) & 1;
    const unsigned trN = hh * 512 + q4 * 64 + g16 * 32 + p4 * 8;
    const unsigned trS = hh * 512 + q4 * 64 + g16 * 32 + (((p4 & 1) << 1) | (p4 >> 1)) * 8;
    bf16x8 A[2][7];
    auto rdA = [&](bf16x8 (&D)[7], const char* b, int ks, int t) {
        const unsigned a = (t < 4 ? trS : trN) + t * 4096 + ks * 1024;
        D[t] = tr_frag(b, a, a + 256);
    };
    auto iterA = [&](auto BSC, int c, int dma, bool lastc) {
        constexpr int BS = decltype(BSC)::value;
        const char* b = lds + (c & 3) * CB;
        const char* bn = lds + ((c + 1) & 3) * CB;
        u32x4(&Bc)[4] = Bf[BS];
        asm volatile("s_waitcnt vmcnt(12)" : "+v"(Bc[0]), "+v"(Bc[1]), "+v"(Bc[2]), "+v"(Bc[3]));
#pragma unroll
        for (int ks = 0; ks < 4; ++ks) {
            if (ks == 2) {
                WAITV(12);
                __builtin_amdgcn_s_barrier();
                SB0();
            }
#pragma unroll
            for (int t = 0; t < 7; ++t) {
                if (ks < 3) rdA(A[(ks + 1) & 1], b, ks + 1, t);
                else if (!lastc) rdA(A[0], bn, 0, t);
                acc[t] = mfma(A[ks & 1][t], __builtin_bit_cast(bf16x8, Bc[ks]), acc[t]);
                if (ks < 2 && (t == 1 || t == 4)) { if (c + 2 < 8) load_b(c + 2, 2 * ks + (t == 4), Bf[(BS + 2) % 3][2 * ks + (t == 4)]); }
                if (ks >= 2 && (t == 1 || t == 4)) {
                    const int p = 2 * (ks - 2) + (t == 4);
                    if (dma == 0) stage_w_p(c + 3, p);
                    else if (dma == 1) stage_kv_p(3 + (p >> 1), p & 1);
                    else if (dma == 2) stage_kv_p(5 + (p >> 1), p & 1);
                    else if (dma == 3) stage_kv_p(7 + (p >> 1), p & 1);
                }
                SB0();
            }
        }
    };
    using std::integral_constant;
    STAMP(0, 1);
    WAITV(12);
    BARRIER();
#pragma unroll
    for (int t = 0; t < 7; ++t) rdA(A[0], lds, 0, t);
    {
    }
    auto iterA0 = [&]() {
        const char* b = lds;
        u32x4(&Bc)[4] = Bf[0];
        asm volatile("" : "+v"(Bc[0]), "+v"(Bc[1]), "+v"(Bc[2]), "+v"(Bc[3]));
#pragma unroll
        for (int ks = 0; ks < 4; ++ks) {
            if (ks == 2) { WAITV(8); __builtin_amdgcn_s_barrier(); SB0(); }
#pragma unroll
            for (int t = 0; t < 7; ++t) {
                if (ks < 3) rdA(A[(ks + 1) & 1], b, ks + 1, t);
                else rdA(A[0], lds + CB, 0, t);
                acc[t] = mfma(A[ks & 1][t], __builtin_bit_cast(bf16x8, Bc[ks]), acc[t]);
                if (ks < 2 && (t == 1 || t == 4)) load_b(2, 2 * ks + (t == 4), Bf[2][2 * ks + (t == 4)]);
                if (ks >= 2 && (t == 1 || t == 4)) stage_w_p(3, 2 * (ks - 2) + (t == 4));
                SB0();
            }
        }
    };
    iterA0();
    iterA(integral_constant<int, 1>{}, 1, 0, false);
    iterA(integral_constant<int, 2>{}, 2, 0, false);
    iterA(integral_constant<int, 0>{}, 3, 0, false);
    iterA(integral_constant<int, 1>{}, 4, 0, false);
    STAMP(0, 2);
    auto iterAT = [&](auto BSC, auto CC) {
        constexpr int BS = decltype(BSC)::value, c = decltype(CC)::value;
        const char* b = lds + (c & 3) * CB;
        const char* bn = lds + ((c + 1) & 3) * CB;
        u32x4(&Bc)[4] = Bf[BS];
        if constexpr (c < 7) asm volatile("s_waitcnt vmcnt(12)" : "+v"(Bc[0]), "+v"(Bc[1]), "+v"(Bc[2]), "+v"(Bc[3]));
        else asm volatile("s_waitcnt vmcnt(8)" : "+v"(Bc[0]), "+v"(Bc[1]), "+v"(Bc[2]), "+v"(Bc[3]));
#pragma unroll
        for (int ks = 0; ks < 4; ++ks) {
            if (ks == 2) {
                if constexpr (c == 5) WAITV(12); else if constexpr (c == 6) WAITV(8); else WAITV(4);
                __builtin_amdgcn_s_barrier();
                SB0();
            }
#pragma unroll
            for (int t = 0; t < 7; ++t) {
                if (ks < 3) rdA(A[(ks + 1) & 1], b, ks + 1, t);
                else if constexpr (c < 7) rdA(A[0], bn, 0, t);
                acc[t] = mfma(A[ks & 1][t], __builtin_bit_cast(bf16x8, Bc[ks]), acc[t]);
                if constexpr (c + 2 < 8) { if (ks < 2 && (t == 1 || t == 4)) load_b(c + 2, 2 * ks + (t == 4), Bf[(BS + 2) % 3][2 * ks + (t == 4)]); }
                if (ks >= 2 && (t == 1 || t == 4)) {
                    const int p = 2 * (ks - 2) + (t == 4);
                    stage_kv_p(2 + 2 * (c - 5) + (p >> 1), p & 1);
                }
                SB0();
            }
        }
    };
    iterAT(integral_constant<int, 2>{}, integral_constant<int, 5>{});
    iterAT(integral_constant<int, 0>{}, integral_constant<int, 6>{});
    iterAT(integral_constant<int, 1>{}, integral_constant<int, 7>{});
    STAMP(0, 3);
    WAITV(4);
    BARRIER();
    stage_kv_p(8, 0); stage_kv_p(8, 1); stage_kv_p(9, 0); stage_kv_p(9, 1);
    bf16x8 qf[8];
    {
#pragma unroll
        for (int t = 4; t < 7; ++t) {
            f32x16 hv;
#pragma unroll
            for (int e = 0; e < 8; ++e) {
                const float g = acc[t][e], u = acc[t][8 + e];
                const float z = g * (1.f + 0.044715f * g * g) * (2.f * 0.7978845608028654f * 1.4426950408889634f);
                hv[e] = g * __builtin_amdgcn_rcpf(1.f + __builtin_amdgcn_exp2f(-z)) * u;
            }
            const int ft = head * 3 + (t - 4);
            if (ft < NFT) hfrag[((size_t)(qb * 8 + wave) * NFT + ft) * 64 + lane] = __builtin_bit_cast(u32x4, pack8(hv, 0));
        }
        const float qs = 0.08838834764831845f * 1.4426950408889634f;
#pragma unroll
        for (int t = 0; t < 4; ++t) {
#pragma unroll
            for (int e = 0; e < 16; ++e) acc[t][e] *= qs;
            qf[2 * t] = pack8(acc[t], 0);
            qf[2 * t + 1] = pack8(acc[t], 1);
        }
    }
    SB0();
    STAMP(0, 4);

    f32x16 o[4];
#pragma unroll
    for (int t = 0; t < 4; ++t) o[t] = f32x16{};
    float m, l = 0.f;
    f32x16 negm;
    const unsigned krd = r * 256 + ((hh ^ (r & 15)) * 16);
    const unsigned vrd = (hh * 4 + q4) * 64 + g16 * 32 + p4 * 8;
    auto rdK = [&](int j, int i) {
        const char* kb = lds + kv_slot(j == 0 ? 0 : 2 * j - 1);
        return row_frag(kb, (krd ^ ((i >> 1) * 32)) + (i & 1) * 8192);
    };
    auto rdV = [&](int j, int i) {
        const char* vb = lds + kv_slot(j == 7 ? 15 : 2 * j + 2);
        const unsigned a = vrd + (2 * (i >> 2) * 4 + (i & 3)) * 512;
        return tr_frag(vb, a, a + 2048);
    };
#define PDV 5
    constexpr int PD = PDV;
    bf16x8 fr[PD + 1];
    f32x16 S[2][2];
    bf16x8 pf[4];
    {
#pragma unroll
        for (int i = 0; i < PD; ++i) fr[i % (PD + 1)] = rdK(0, i);
#pragma unroll
        for (int i = 0; i < 16; ++i) {
            if (i + PD < 16) fr[(i + PD) % (PD + 1)] = rdK(0, i + PD);
            else fr[(i + PD) % (PD + 1)] = rdK(1, i + PD - 16);
            S[0][i & 1] = mfma(fr[i % (PD + 1)], qf[i >> 1], i < 2 ? f32x16{} : S[0][i & 1]);
            SB0();
        }
        float pm = max3(S[0][0][0], S[0][0][1], S[0][1][0]);
#pragma unroll
        for (int e = 2; e < 16; ++e) pm = fmaxf(pm, S[0][0][e]);
#pragma unroll
        for (int e = 1; e < 16; ++e) pm = fmaxf(pm, S[0][1][e]);
        m = max32(pm);
#pragma unroll
        for (int e = 0; e < 16; ++e) { S[0][0][e] -= m; S[0][1][e] -= m; negm[e] = -m; }
    }
    auto iterB = [&](auto JC, auto DC) {
        constexpr int j = decltype(JC)::value;
        constexpr int dma0 = decltype(DC)::value;
        constexpr int c = j & 1, n = c ^ 1;
        constexpr int g0 = 16 + 32 * j;
        float ps0 = 0.f, ps1 = 0.f;
#pragma unroll
        for (int i = 0; i < 16; ++i) {
            if constexpr (j < 7) {
                if (i + PD < 16) fr[(g0 + i + PD) % (PD + 1)] = rdK(j + 1, i + PD);
                else fr[(g0 + i + PD) % (PD + 1)] = rdV(j, i + PD - 16);
                S[n][i & 1] = mfma(fr[(g0 + i) % (PD + 1)], qf[i >> 1], i < 2 ? negm : S[n][i & 1]);
            }
            S[c][0][i] = __builtin_amdgcn_exp2f(S[c][0][i]);
            if (i & 1) ps1 += S[c][0][i]; else ps0 += S[c][0][i];
            if ((i & 7) == 7) pf[i >> 3] = pack8(S[c][0], i >> 3);
            if constexpr (dma0 != 0) { if (i == 3 || i == 11) stage_kv_p(dma0, i >> 3); }
            SB0();
        }
        constexpr int g1 = (j < 7) ? g0 + 16 : g0;
        float pm = -3e38f;
#pragma unroll
        for (int i = 0; i < 16; ++i) {
            if (i + PD < 16) fr[(g1 + i + PD) % (PD + 1)] = rdV(j, i + PD);
            else if constexpr (j < 6) fr[(g1 + i + PD) % (PD + 1)] = rdK(j + 2, i + PD - 16);
            else if constexpr (j == 6) fr[(g1 + i + PD) % (PD + 1)] = rdV(7, i + PD - 16);
            if (i < 8) {
                S[c][1][2 * i] = __builtin_amdgcn_exp2f(S[c][1][2 * i]);
                S[c][1][2 * i + 1] = __builtin_amdgcn_exp2f(S[c][1][2 * i + 1]);
                ps0 += S[c][1][2 * i];
                ps1 += S[c][1][2 * i + 1];
                if (i == 3) pf[2] = pack8(S[c][1], 0);
                if (i == 7) pf[3] = pack8(S[c][1], 1);
            }
            o[i & 3] = mfma(fr[(g1 + i) % (PD + 1)], pf[i >> 2], o[i & 3]);
            if constexpr (j < 7) { if (i >= 8) { pm = max3(pm, S[n][(i - 8) >> 2][(4 * (i - 8)) & 15], S[n][(i - 8) >> 2][((4 * (i - 8)) & 15) + 1]); pm = max3(pm, S[n][(i - 8) >> 2][((4 * (i - 8)) & 15) + 2], S[n][(i - 8) >> 2][((4 * (i - 8)) & 15) + 3]); } }
            if constexpr (dma0 != 0 && dma0 + 1 <= 15) { if (i == 3 || i == 11) stage_kv_p(dma0 + 1, i >> 3); }
            SB0();
        }
        l += ps0 + ps1;
        if constexpr (j < 7) {
            pm = max32(pm);
            if (__builtin_expect(__any(pm > 8.f), 0)) {
                const float d = fmaxf(pm, 0.f);
                const float al = __builtin_amdgcn_exp2f(-d);
                m += d;
                l *= al;
#pragma unroll
                for (int e = 0; e < 16; ++e) { S[n][0][e] -= d; S[n][1][e] -= d; negm[e] = -m; }
#pragma unroll
                for (int t = 0; t < 4; ++t)
#pragma unroll
                    for (int e = 0; e < 16; ++e) o[t][e] *= al;
            }
        }
    };
#define RAWBAR() do { __builtin_amdgcn_s_barrier(); SB0(); } while (0)
    iterB(integral_constant<int, 0>{}, integral_constant<int, 0>{});
    STAMP(0, 5);
    RAWBAR();
    iterB(integral_constant<int, 1>{}, integral_constant<int, 10>{});
    STAMP(0, 6);
    WAITV(8); RAWBAR();
    iterB(integral_constant<int, 2>{}, integral_constant<int, 12>{});
    STAMP(0, 7);
    WAITV(8); RAWBAR();
    iterB(integral_constant<int, 3>{}, integral_constant<int, 14>{});
    unsigned pfd = 0;
    {
        const int sp2 = xcd & 3;
        int row = idx * 43 + wave * 8 + (lane >> 3);
        row = row < idx * 43 + 43 ? row : idx * 43 + 42;
        int grow = (sp2 < 3 ? sp2 * 1376 : 0) + row;
        grow = grow < (sp2 < 3 ? NH * DH : FF) ? grow : (sp2 < 3 ? NH * DH : FF) - 1;
        const char* pp = (const char*)(sp2 < 3 ? wo : wd) + (size_t)grow * (DM * 2) + (lane & 7) * 128;
        if (wave < 6) asm volatile("global_load_ushort %0, %1, off" : "=v"(pfd) : "v"(pp));
    }
    STAMP(0, 8);
    WAITV(8); RAWBAR();
    iterB(integral_constant<int, 4>{}, integral_constant<int, 0>{});
    STAMP(0, 9);
    WAITV(4); RAWBAR();
    iterB(integral_constant<int, 5>{}, integral_constant<int, 0>{});
    STAMP(0, 10);
    WAITV(0); RAWBAR();
    asm volatile("" ::"v"(pfd));
    iterB(integral_constant<int, 6>{}, integral_constant<int, 0>{});
    STAMP(0, 11);
    iterB(integral_constant<int, 7>{}, integral_constant<int, 0>{});
    STAMP(0, 12);

    l = sum32(l);
    const float rl = 1.f / l;
    const unsigned lane2 = lane_id_opaque();
#pragma unroll
    for (int t = 0; t < 4; ++t) {
#pragma unroll
        for (int e = 0; e < 16; ++e) o[t][e] *= rl;
        u32x4* dst = ofrag + ((size_t)(qb * 8 + wave) * 256 + head * 8 + 2 * t) * 64 + lane2;
        dst[0] = __builtin_bit_cast(u32x4, pack8(o[t], 0));
        dst[64] = __builtin_bit_cast(u32x4, pack8(o[t], 1));
    }
    STAMP(0, 13);
    RSTAMP(0, 31);
    STAMP_FLUSH(0, 14);
}

constexpr unsigned K2_STG = 32768;
constexpr unsigned K2_LDS = 4 * K2_STG + 1024;

__global__ __launch_bounds__(512, 2) void k2_out(const __hip_bfloat16* __restrict__ x, const __hip_bfloat16* __restrict__ wo,
                                                 const __hip_bfloat16* __restrict__ wd, const u32x4* __restrict__ ofrag,
                                                 const u32x4* __restrict__ hfrag, float* __restrict__ part, unsigned* __restrict__ cnt,
                                                 __hip_bfloat16* __restrict__ out) {
    extern __shared__ __attribute__((aligned(16))) char lds[];
    const int tid = threadIdx.x, lane = tid & 63;
    const int wave = __builtin_amdgcn_readfirstlane(tid >> 6);
    const int r = lane & 31, hh = lane >> 5;
    const int bid = blockIdx.x, xcd = bid & 7, idx = bid >> 3;
    const int sp = xcd & 3, tq = (xcd >> 2) * 8 + (idx >> 2), tn = idx & 3;
    const int tile = tq * 4 + tn;
    const int g = wave & 3, kh = wave >> 2;
    STAMP_DECL;
    STAMP(1, 0);
    u32x2 xres[2];
    {
        const char* xp = (const char*)x + ((size_t)(tq * 128 + sp * 32 + r) * DM + tn * 128 + (wave >> 1) * 32 + 16 * (wave & 1) + 4 * hh) * 2;
        asm volatile("global_load_dwordx2 %0, %2, off\n\tglobal_load_dwordx2 %1, %2, off offset:16" : "=&v"(xres[0]), "=&v"(xres[1]) : "v"(xp));
    }
    const int s0 = sp < 3 ? sp * 86 : 0;
    const int ns = sp == 2 ? 84 : 86;
    const int ktot = sp < 3 ? NH * DH : FF;
    const char* wbase = (const char*)(sp < 3 ? wo : wd);
    const unsigned ldsb = (unsigned)(size_t)(LAS char*)lds;
    const char* bbase = (const char*)(sp < 3 ? ofrag + ((size_t)(tq * 4 + g) * 256 + s0) * 64 : hfrag + (size_t)(tq * 4 + g) * NFT * 64);
    const unsigned boff = lane * 16;
    const int wrow = s0 * 16 + wave * 16 + ((lane >> 2) & 7);
    const unsigned wcol = (tn * 128 + (lane >> 5) * 32 + (lane & 3) * 8) * 2;
    const int rot2 = 0;
    auto phys = [&](int c) { int q = c + rot2; q = q >= 10 ? q - 10 : q; return c < 10 ? q : 10; };
    auto stage_w = [&](int c, int half) {
        int r0 = wrow + phys(c) * 128 + half * 8;
        r0 = r0 < ktot ? r0 : ktot - 1;
        const unsigned a0 = r0 * (DM * 2) + wcol;
        const unsigned o_ = ldsb + (c & 3) * K2_STG + wave * 4096 + half * 2048;
        glds16s(wbase, a0, o_);
        glds16s(wbase + 128, a0, o_ + 1024);
    };
    f32x16 acc[4];
#pragma unroll
    for (int t = 0; t < 4; ++t) acc[t] = f32x16{};
    const int q4 = (lane & 15) >> 2, p4 = lane & 3, g16 = (lane >> 4) & 1;
    const unsigned trd = (hh * 4 + q4) * 64 + g16 * 32 + p4 * 8 + kh * 4096;
    u32x4 bf[4][4];
    auto load_b = [&](int c, int i) {
        int s = 8 * phys(c) + 2 * i + kh;
        s = s < ns ? s : ns - 1;
        const char* sb = bbase + (size_t)s * 1024;
        asm volatile("global_load_dwordx4 %0, %1, %2" : "=v"(bf[c & 3][i]) : "v"(boff), "s"(sb));
    };
    auto rdW = [&](int c, int i) {
        const unsigned a = trd + (i >> 2) * 8192 + (i & 3) * 512;
        return tr_frag(lds + (c & 3) * K2_STG, a, a + 2048);
    };
    constexpr int PD = 6;
    bf16x8 fr[PD + 1];
#pragma unroll
    for (int c = 0; c < 3; ++c) {
        stage_w(c, 0); stage_w(c, 1);
        load_b(c, 0); load_b(c, 1); load_b(c, 2); load_b(c, 3);
    }
    auto stage_iter = [&](auto CC) {
        constexpr int c = decltype(CC)::value;
        u32x4(&b)[4] = bf[c & 3];
        if constexpr (c == 0) asm volatile("s_waitcnt vmcnt(16)" : "+v"(b[0]), "+v"(b[1]), "+v"(b[2]), "+v"(b[3]), "+v"(xres[0]), "+v"(xres[1])::"memory");
        else if constexpr (c < 9) asm volatile("s_waitcnt vmcnt(16)" : "+v"(b[0]), "+v"(b[1]), "+v"(b[2]), "+v"(b[3])::"memory");
        else if constexpr (c == 9) asm volatile("s_waitcnt vmcnt(8)" : "+v"(b[0]), "+v"(b[1]), "+v"(b[2]), "+v"(b[3])::"memory");
        else asm volatile("s_waitcnt vmcnt(0)" : "+v"(b[0]), "+v"(b[1]), "+v"(b[2]), "+v"(b[3])::"memory");
        BARRIER();
        STAMP(1, 1 + c);
#pragma unroll
        for (int i = 0; i < PD; ++i) fr[i % (PD + 1)] = rdW(c, i);
#pragma unroll
        for (int i = 0; i < 16; ++i) {
            if (i + PD < 16) fr[(i + PD) % (PD + 1)] = rdW(c, i + PD);
            if (c < 10 || 8 * c + 2 * (i >> 2) + kh < ns) acc[i & 3] = mfma(fr[i % (PD + 1)], __builtin_bit_cast(bf16x8, b[i >> 2]), acc[i & 3]);
            if constexpr (c + 3 < 11) {
                if (i == 0) stage_w(c + 3, 0);
                if (i == 2) stage_w(c + 3, 1);
                if (i == 4) load_b(c + 3, 0);
                if (i == 6) load_b(c + 3, 1);
                if (i == 8) load_b(c + 3, 2);
                if (i == 10) load_b(c + 3, 3);
            }
            SB0();
        }
    };
    using std::integral_constant;
    stage_iter(integral_constant<int, 0>{});
    stage_iter(integral_constant<int, 1>{});
    stage_iter(integral_constant<int, 2>{});
    stage_iter(integral_constant<int, 3>{});
    stage_iter(integral_constant<int, 4>{});
    stage_iter(integral_constant<int, 5>{});
    stage_iter(integral_constant<int, 6>{});
    stage_iter(integral_constant<int, 7>{});
    stage_iter(integral_constant<int, 8>{});
    stage_iter(integral_constant<int, 9>{});
    stage_iter(integral_constant<int, 10>{});
    BARRIER();
    STAMP(1, 12);
    if (kh == 1) {
#pragma unroll
        for (int t = 0; t < 4; ++t)
#pragma unroll
            for (int qd = 0; qd < 4; ++qd) {
                f32x4 v = {acc[t][4 * qd], acc[t][4 * qd + 1], acc[t][4 * qd + 2], acc[t][4 * qd + 3]};
                *(f32x4*)(lds + g * 16384 + (t * 4 + qd) * 1024 + lane * 16) = v;
            }
    }
    BARRIER();
    const __amdgpu_buffer_rsrc_t prs = __builtin_amdgcn_make_buffer_rsrc(part, 0, 64 * 4 * 65536, 0x00020000);
    if (kh == 0) {
#pragma unroll
        for (int t = 0; t < 4; ++t)
#pragma unroll
            for (int qd = 0; qd < 4; ++qd) {
                f32x4 v = *(const f32x4*)(lds + g * 16384 + (t * 4 + qd) * 1024 + lane * 16);
                v[0] += acc[t][4 * qd]; v[1] += acc[t][4 * qd + 1]; v[2] += acc[t][4 * qd + 2]; v[3] += acc[t][4 * qd + 3];
                if (g != sp) __builtin_amdgcn_raw_buffer_store_b128(__builtin_bit_cast(u32x4, v), prs, ((tile * 4 + sp) * 4 + g) * 16384 + (t * 4 + qd) * 1024 + lane * 16, 0, 16);
                else *(f32x4*)(lds + 65536 + (t * 4 + qd) * 1024 + lane * 16) = v;
            }
    }
    STAMP(1, 13);
    WAITV(0);
    BARRIER();
    STAMP(1, 14);
    if (tid == 0) {
        __hip_atomic_fetch_add((GAS unsigned*)(cnt + tile * 16), 1u, __ATOMIC_RELAXED, __HIP_MEMORY_SCOPE_AGENT);
        unsigned spins = 0;
        while (__hip_atomic_load((GAS unsigned*)(cnt + tile * 16), __ATOMIC_RELAXED, __HIP_MEMORY_SCOPE_AGENT) < 4u) {
            __builtin_amdgcn_s_sleep(1);
            if (++spins > (1u << 22)) break;
        }
    }
    BARRIER();
    STAMP(1, 15);
    {
        const int t = wave >> 1, q0 = 2 * (wave & 1);
        const int pi = t * 4 + q0;
        f32x4 f[3][2];
#pragma unroll
        for (int k = 0; k < 3; ++k) {
            const int src = k + (k >= sp ? 1 : 0);
#pragma unroll
            for (int qd = 0; qd < 2; ++qd)
                f[k][qd] = __builtin_bit_cast(f32x4, __builtin_amdgcn_raw_buffer_load_b128(prs, ((tile * 4 + src) * 4 + sp) * 16384 + (pi + qd) * 1024 + lane * 16, 0, 16));
        }
        const int row = tq * 128 + sp * 32 + r;
#pragma unroll
        for (int qd = 0; qd < 2; ++qd) {
            const f32x4 own = *(const f32x4*)(lds + 65536 + (pi + qd) * 1024 + lane * 16);
            const f32x4 sv = (f[0][qd] + f[1][qd]) + (sp == 3 ? f[2][qd] : own);
            const f32x4 gv = sp == 3 ? own : f[2][qd];
            const int n = tn * 128 + t * 32 + 8 * (q0 + qd) + 4 * hh;
            const u32x2 xv = xres[qd];
            float xf[4] = {__uint_as_float(xv[0] << 16), __uint_as_float(xv[0] & 0xffff0000u), __uint_as_float(xv[1] << 16), __uint_as_float(xv[1] & 0xffff0000u)};
            float ov[4];
#pragma unroll
            for (int e = 0; e < 4; ++e) {
                const float s = bfr(sv[e]);
                const float gg = bfr(gv[e]);
                const float sg = bfr(gg * __builtin_amdgcn_rcpf(1.f + __builtin_amdgcn_exp2f(-gg * 1.4426950408889634f)));
                ov[e] = xf[e] + bfr(s * sg);
            }
            u32x2 w = {cvtpk(ov[0], ov[1]), cvtpk(ov[2], ov[3])};
            *(u32x2*)((char*)out + ((size_t)row * DM + n) * 2) = w;
        }
    }
    STAMP(1, 16);
    STAMP(1, 17);
    STAMP_FLUSH(1, 18);
}

extern "C" void kernel_launch(void* const* d_in, const int* in_sizes, int n_in, void* d_out, int out_size, void* d_ws, size_t ws_size,
                              hipStream_t stream) {
    const __hip_bfloat16* x = (const __hip_bfloat16*)d_in[0];
    const __hip_bfloat16* kc = (const __hip_bfloat16*)d_in[1];
    const __hip_bfloat16* vc = (const __hip_bfloat16*)d_in[2];
    const __hip_bfloat16* wq = (const __hip_bfloat16*)d_in[3];
    const __hip_bfloat16* wo = (const __hip_bfloat16*)d_in[4];
    const __hip_bfloat16* wg = (const __hip_bfloat16*)d_in[6];
    const __hip_bfloat16* wu = (const __hip_bfloat16*)d_in[7];
    const __hip_bfloat16* wd = (const __hip_bfloat16*)d_in[8];
    char* ws = (char*)d_ws;
    u32x4* ofrag = (u32x4*)ws;
    u32x4* hfrag = (u32x4*)(ws + (16u << 20));
    float* part = (float*)(ws + (24u << 20));
    unsigned* cnt = (unsigned*)(ws + (40u << 20));
    u32x4* xfrag = (u32x4*)(ws + (41u << 20));
    (void)hipFuncSetAttribute((const void*)k1_attn, hipFuncAttributeMaxDynamicSharedMemorySize, K1_LDS);
    (void)hipFuncSetAttribute((const void*)k2_out, hipFuncAttributeMaxDynamicSharedMemorySize, K2_LDS);
    k0_ln<<<256, 256, 0, stream>>>(x, xfrag, cnt, wq, wg, wu);
    k1_attn<<<256, 512, K1_LDS, stream>>>(xfrag, kc, vc, wq, wg, wu, ofrag, hfrag, wo, wd);
    k2_out<<<256, 512, K2_LDS, stream>>>(x, wo, wd, ofrag, hfrag, part, cnt, (__hip_bfloat16*)d_out);
}
```

```cpp
#include <hip/hip_runtime.h>
#include <hip/hip_bf16.h>
#include <stdint.h>

typedef __bf16 bf16x8 __attribute__((ext_vector_type(8)));
typedef __bf16 bf16x2 __attribute__((ext_vector_type(2)));
typedef short s16x4 __attribute__((ext_vector_type(4)));
typedef short s16x8 __attribute__((ext_vector_type(8)));
typedef float f32x16 __attribute__((ext_vector_type(16)));
typedef float f32x4 __attribute__((ext_vector_type(4)));
typedef float f32x2 __attribute__((ext_vector_type(2)));
typedef unsigned u32x4 __attribute__((ext_vector_type(4)));
typedef unsigned u32x2 __attribute__((ext_vector_type(2)));
#define LAS __attribute__((address_space(3)))
#define GAS __attribute__((address_space(1)))
#define DI __device__ __forceinline__

constexpr int SEQ = 2048, DM = 512, NH = 32, DH = 128, WIN = 512, KOFF = SEQ - WIN, FF = 1376;
constexpr int NFT = FF / 16;

DI void glds16(const void* g, unsigned ldsaddr) {
    asm volatile("s_mov_b32 m0, %0\n\ts_nop 0\n\tglobal_load_lds_dwordx4 %1, off" ::"s"(ldsaddr), "v"(g));
}
DI void glds16s(const void* sbase, unsigned voff, unsigned ldsaddr) {
    asm volatile("s_mov_b32 m0, %0\n\ts_nop 3\n\tglobal_load_lds_dwordx4 %1, %2" ::"s"(ldsaddr), "v"(voff), "s"(sbase));
}
#define WAITV(n) asm volatile("s_waitcnt vmcnt(" #n ")" ::: "memory")
#define WAITL0() asm volatile("s_waitcnt lgkmcnt(0)" ::: "memory")
#define SB0() __builtin_amdgcn_sched_barrier(0)
#define BARRIER() do { SB0(); WAITL0(); __builtin_amdgcn_s_barrier(); asm volatile("" ::: "memory"); SB0(); } while (0)
DI unsigned lane_id_opaque() {
    unsigned l;
    asm volatile("v_mbcnt_lo_u32_b32 %0, -1, 0\n\tv_mbcnt_hi_u32_b32 %0, -1, %0" : "=v"(l));
    return l;
}

DI bf16x8 tr_frag(const char* lds, unsigned a0, unsigned a1) {
    s16x4 lo = __builtin_amdgcn_ds_read_tr16_b64_v4i16((LAS s16x4*)(lds + a0));
    s16x4 hi = __builtin_amdgcn_ds_read_tr16_b64_v4i16((LAS s16x4*)(lds + a1));
    s16x8 v = __builtin_shufflevector(lo, hi, 0, 1, 2, 3, 4, 5, 6, 7);
    return __builtin_bit_cast(bf16x8, v);
}
DI bf16x8 row_frag(const char* lds, unsigned a) { return *(const LAS bf16x8*)(lds + a); }
DI f32x16 mfma(bf16x8 a, bf16x8 b, f32x16 c) { return __builtin_amdgcn_mfma_f32_32x32x16_bf16(a, b, c, 0, 0, 0); }
DI unsigned cvtpk(float lo, float hi) {
    f32x2 v = {lo, hi};
    bf16x2 b = __builtin_convertvector(v, bf16x2);
    return __builtin_bit_cast(unsigned, b);
}
DI float bfr(float x) {
    return __uint_as_float(cvtpk(x, x) << 16);
}
DI float dot2(unsigned a, unsigned b, float c) {
    return __builtin_amdgcn_fdot2_f32_bf16(__builtin_bit_cast(bf16x2, a), __builtin_bit_cast(bf16x2, b), c, false);
}
DI float sum32(float x) {
    auto rr = __builtin_amdgcn_permlane32_swap(__float_as_uint(x), __float_as_uint(x), false, false);
    return __uint_as_float(rr[0]) + __uint_as_float(rr[1]);
}
DI float max32(float x) {
    auto rr = __builtin_amdgcn_permlane32_swap(__float_as_uint(x), __float_as_uint(x), false, false);
    return fmaxf(__uint_as_float(rr[0]), __uint_as_float(rr[1]));
}
DI float max3(float a, float b, float c) { return __builtin_fmaxf(__builtin_fmaxf(a, b), c); }
DI bf16x8 pack8(const f32x16& x, int s) {
    u32x4 p = {cvtpk(x[8 * s + 0], x[8 * s + 1]), cvtpk(x[8 * s + 2], x[8 * s + 3]), cvtpk(x[8 * s + 4], x[8 * s + 5]), cvtpk(x[8 * s + 6], x[8 * s + 7])};
    return __builtin_bit_cast(bf16x8, p);
}

#define STAMP(k, i)
#define RSTAMP(k, i)
#define STAMP_DECL
#define STAMP_FLUSH(k, n)

__global__ __launch_bounds__(256) void k0_ln(const __hip_bfloat16* __restrict__ x, u32x4* __restrict__ xfrag, unsigned* __restrict__ cnt,
                                             const __hip_bfloat16* __restrict__ wq, const __hip_bfloat16* __restrict__ wg,
                                             const __hip_bfloat16* __restrict__ wu) {
    const int tid = threadIdx.x, lane = tid & 63, i = lane & 31;
    const int row = blockIdx.x * 8 + (tid >> 5);
    if (blockIdx.x == 0 && tid < 64) cnt[tid * 16] = 0u;
    const u32x4* src = (const u32x4*)((const char*)x + (size_t)row * (DM * 2) + i * 32);
    const u32x4 a = src[0], b = src[1];
    unsigned short pf = 0;
    {
        const int xcd = blockIdx.x & 7, idx = blockIdx.x >> 3, wave = tid >> 6;
        const char* pp;
        if (wave < 2) pp = (const char*)wq + (size_t)(idx * 16 + wave * 8 + (lane >> 3)) * (NH * DH * 2) + xcd * 1024 + (lane & 7) * 128;
        else {
            int cb = xcd * 384 + (lane & 3) * 128;
            cb = cb < FF * 2 - 2 ? cb : FF * 2 - 2;
            pp = (const char*)(wave == 2 ? wg : wu) + (size_t)(idx * 16 + (lane >> 2)) * (FF * 2) + cb;
        }
        pf = *(const volatile unsigned short*)pp;
    }
    float v[16];
#pragma unroll
    for (int e = 0; e < 4; ++e) {
        v[2 * e] = __uint_as_float(a[e] << 16); v[2 * e + 1] = __uint_as_float(a[e] & 0xffff0000u);
        v[8 + 2 * e] = __uint_as_float(b[e] << 16); v[8 + 2 * e + 1] = __uint_as_float(b[e] & 0xffff0000u);
    }
    float s = 0.f;
#pragma unroll
    for (int e = 0; e < 16; ++e) s += v[e];
#pragma unroll
    for (int o = 1; o < 32; o <<= 1) s += __shfl_xor(s, o);
    const float mean = s * (1.f / DM);
    float q = 0.f;
#pragma unroll
    for (int e = 0; e < 16; ++e) { const float d = v[e] - mean; q = fmaf(d, d, q); }
#pragma unroll
    for (int o = 1; o < 32; o <<= 1) q += __shfl_xor(q, o);
    const float mb = bfr(mean);
    const float den = bfr(__builtin_sqrtf(bfr(bfr(q * (1.f / DM)) + 1e-5f)));
    u32x4 o0, o1;
#pragma unroll
    for (int e = 0; e < 4; ++e) {
        o0[e] = cvtpk(bfr(v[2 * e] - mb) / den, bfr(v[2 * e + 1] - mb) / den);
        o1[e] = cvtpk(bfr(v[8 + 2 * e] - mb) / den, bfr(v[8 + 2 * e + 1] - mb) / den);
    }
    u32x4* dst = xfrag + ((size_t)(row >> 5) * 32 + i) * 64 + (row & 31);
    dst[0] = o0;
    dst[32] = o1;
    asm volatile("" ::"v"((unsigned)pf));
}

constexpr unsigned CB = 28672;
constexpr unsigned K1_EXT = 4 * CB;
constexpr unsigned K1_LDS = 163840;
DI constexpr unsigned kv_slot(int i) {
    const int s = i % 10;
    return s < 3 ? K1_EXT + 16384u * s : 16384u * (s - 3);
}

template <bool NEAR>
__global__ __launch_bounds__(512, 2) void k1_attn(const u32x4* __restrict__ xfrag, const __hip_bfloat16* __restrict__ kc,
                                                  const __hip_bfloat16* __restrict__ vc, const __hip_bfloat16* __restrict__ wq,
                                                  const __hip_bfloat16* __restrict__ wg, const __hip_bfloat16* __restrict__ wu,
                                                  u32x4* __restrict__ ofrag, u32x4* __restrict__ hfrag,
                                                  const __hip_bfloat16* __restrict__ wo, const __hip_bfloat16* __restrict__ wd) {
    extern __shared__ __attribute__((aligned(16))) char lds[];
    const int tid = threadIdx.x, lane = tid & 63;
    const int wave = __builtin_amdgcn_readfirstlane(tid >> 6);
    const int r = lane & 31, hh = lane >> 5;
    const int bid = blockIdx.x, xcd = bid & 7, idx = bid >> 3;
    const int head = xcd * 4 + (idx & 3), qb = idx >> 2;
    STAMP_DECL;
    STAMP(0, 0);
    RSTAMP(0, 30);

    const unsigned ldsb = (unsigned)(size_t)(LAS char*)lds;
    const int rot = 0, rotk = 0;
    const char* xbase = (const char*)(xfrag + (size_t)(qb * 8 + wave) * 32 * 64);
    const unsigned xoff = lane * 16;
    const char* kbase = (const char*)kc + ((size_t)head * SEQ + KOFF) * (DH * 2);
    const char* vbase = (const char*)vc + ((size_t)head * SEQ + KOFF) * (DH * 2);
    const unsigned kp0 = (lane >> 4) * (DH * 2) + (((lane & 15) ^ (lane >> 4)) * 16);
    const char* wsb;
    unsigned wv;
    const char* wp = nullptr;
    int wps, wst, wxm;
    {
        const int krow = 8 * (lane >> 5) + ((lane >> 2) & 7), ch = lane & 3;
        if (wave < 4) {
            const int wld = NH * DH * 2;
            wsb = (const char*)wq + (head * DH + wave * 32) * 2;
            wv = krow * wld + ch * 16;
            wps = 16 * wld; wst = 64 * wld; wxm = 0;
        } else if (wave < 7) {
            const int wld = FF * 2;
            int ft = head * 3 + (wave - 4);
            ft = ft < NFT ? ft : NFT - 1;
            const char* lo = (const char*)wg < (const char*)wu ? (const char*)wg : (const char*)wu;
            wsb = lo + ft * 32;
            wv = krow * wld + (ch & 1) * 16 + (unsigned)((ch < 2 ? (const char*)wg : (const char*)wu) - lo);
            wps = 16 * wld; wst = 64 * wld; wxm = 0;
            if (!NEAR) { wsb = (const char*)nullptr; wp = ((ch < 2) ? (const char*)wg : (const char*)wu) + (size_t)krow * wld + (ft * 16 + (ch & 1) * 8) * 2; }
        } else {
            wsb = kbase;
            wv = kp0;
            wps = 1024; wst = 4096; wxm = 64;
        }
        if (!NEAR && !(wave >= 4 && wave < 7)) { wp = wsb + wv; }
    }
    const unsigned koff = kp0 ^ (((2 * wave) & 3) << 6);
    const unsigned voff = (wave * 8 + ((lane >> 2) & 7)) * (DH * 2) + ((lane >> 5) * 32 + (lane & 3) * 8) * 2;
    const unsigned wdst = wave < 7 ? wave * 4096 : 0;
    auto stage_w_p = [&](int c, int p) {
        const unsigned dst = ldsb + (wave < 7 ? (c & 3) * CB + wdst : K1_EXT + c * 4096) + p * 1024;
        if constexpr (NEAR) glds16s(wsb + (size_t)c * wst + p * wps, wv ^ (unsigned)(wxm * p), dst);
        else glds16(wp + (size_t)c * wst + p * wps + (int)((wv ^ (unsigned)(wxm * p)) - wv), dst);
    };
    auto stage_kv_p = [&](int i, int p) {
        const unsigned so = ldsb + kv_slot(i) + wave * 2048 + p * 1024;
        const bool isk = (i == 0) || ((i & 1) && i < 15);
        if (isk) {
            const int j = ((((i + 1) >> 1)) + rotk) & 7;
            glds16s(kbase + (size_t)j * 64 * (DH * 2) + wave * 2048 + p * 1024, koff ^ (p * 64), so);
        } else {
            const int j = (((i == 15) ? 7 : ((i - 2) >> 1)) + rotk) & 7;
            glds16s(vbase + (size_t)j * 64 * (DH * 2) + p * 128, voff, so);
        }
    };
    u32x4 Bf[3][4];
    auto load_b = [&](int c, int ks, u32x4& dst) {
        asm volatile("s_nop 4\n\tglobal_load_dwordx4 %0, %1, %2" : "=v"(dst) : "v"(xoff), "s"(xbase + (size_t)((((c + rot) & 7) * 4 + ks) * 1024)));
    };
#pragma unroll
    for (int ks = 0; ks < 4; ++ks) load_b(0, ks, Bf[0][ks]);
#pragma unroll
    for (int p = 0; p < 4; ++p) stage_w_p(0, p);
#pragma unroll
    for (int ks = 0; ks < 4; ++ks) load_b(1, ks, Bf[1][ks]);
#pragma unroll
    for (int p = 0; p < 4; ++p) stage_w_p(1, p);
#pragma unroll
    for (int p = 0; p < 4; ++p) stage_w_p(2, p);

    f32x16 acc[7];
#pragma unroll
    for (int t = 0; t < 7; ++t) acc[t] = f32x16{};
    const int q4 = (lane & 15) >> 2, p4 = lane & 3, g16 = (lane >> 4) & 1;
    const unsigned trN = hh * 512 + q4 * 64 + g16 * 32 + p4 * 8;
    const unsigned trS = hh * 512 + q4 * 64 + g16 * 32 + (((p4 & 1) << 1) | (p4 >> 1)) * 8;
    bf16x8 A[2][7];
    auto rdA = [&](bf16x8 (&D)[7], const char* b, int ks, int t) {
        const unsigned a = (t < 4 ? trS : trN) + t * 4096 + ks * 1024;
        D[t] = tr_frag(b, a, a + 256);
    };
    auto iterA = [&](auto BSC, int c, int dma, bool lastc) {
        constexpr int BS = decltype(BSC)::value;
        const char* b = lds + (c & 3) * CB;
        const char* bn = lds + ((c + 1) & 3) * CB;
        u32x4(&Bc)[4] = Bf[BS];
        asm volatile("s_waitcnt vmcnt(12)" : "+v"(Bc[0]), "+v"(Bc[1]), "+v"(Bc[2]), "+v"(Bc[3]));
#pragma unroll
        for (int ks = 0; ks < 4; ++ks) {
            if (ks == 2) {
                WAITV(12);
                __builtin_amdgcn_s_barrier();
                SB0();
            }
#pragma unroll
            for (int t = 0; t < 7; ++t) {
                if (ks < 3) rdA(A[(ks + 1) & 1], b, ks + 1, t);
                else if (!lastc) rdA(A[0], bn, 0, t);
                acc[t] = mfma(A[ks & 1][t], __builtin_bit_cast(bf16x8, Bc[ks]), acc[t]);
                if (ks < 2 && (t == 1 || t == 4)) { if (c + 2 < 8) load_b(c + 2, 2 * ks + (t == 4), Bf[(BS + 2) % 3][2 * ks + (t == 4)]); }
                if (ks >= 2 && (t == 1 || t == 4)) {
                    const int p = 2 * (ks - 2) + (t == 4);
                    if (dma == 0) stage_w_p(c + 3, p);
                    else if (dma == 1) stage_kv_p(3 + (p >> 1), p & 1);
                    else if (dma == 2) stage_kv_p(5 + (p >> 1), p & 1);
                    else if (dma == 3) stage_kv_p(7 + (p >> 1), p & 1);
                }
                SB0();
            }
        }
    };
    using std::integral_constant;
    STAMP(0, 1);
    WAITV(12);
    BARRIER();
#pragma unroll
    for (int t = 0; t < 7; ++t) rdA(A[0], lds, 0, t);
    {
    }
    auto iterA0 = [&]() {
        const char* b = lds;
        u32x4(&Bc)[4] = Bf[0];
        asm volatile("" : "+v"(Bc[0]), "+v"(Bc[1]), "+v"(Bc[2]), "+v"(Bc[3]));
#pragma unroll
        for (int ks = 0; ks < 4; ++ks) {
            if (ks == 2) { WAITV(8); __builtin_amdgcn_s_barrier(); SB0(); }
#pragma unroll
            for (int t = 0; t < 7; ++t) {
                if (ks < 3) rdA(A[(ks + 1) & 1], b, ks + 1, t);
                else rdA(A[0], lds + CB, 0, t);
                acc[t] = mfma(A[ks & 1][t], __builtin_bit_cast(bf16x8, Bc[ks]), acc[t]);
                if (ks < 2 && (t == 1 || t == 4)) load_b(2, 2 * ks + (t == 4), Bf[2][2 * ks + (t == 4)]);
                if (ks >= 2 && (t == 1 || t == 4)) stage_w_p(3, 2 * (ks - 2) + (t == 4));
                SB0();
            }
        }
    };
    iterA0();
    iterA(integral_constant<int, 1>{}, 1, 0, false);
    iterA(integral_constant<int, 2>{}, 2, 0, false);
    iterA(integral_constant<int, 0>{}, 3, 0, false);
    iterA(integral_constant<int, 1>{}, 4, 0, false);
    STAMP(0, 2);
    auto iterAT = [&](auto BSC, auto CC) {
        constexpr int BS = decltype(BSC)::value, c = decltype(CC)::value;
        const char* b = lds + (c & 3) * CB;
        const char* bn = lds + ((c + 1) & 3) * CB;
        u32x4(&Bc)[4] = Bf[BS];
        if constexpr (c < 7) asm volatile("s_waitcnt vmcnt(12)" : "+v"(Bc[0]), "+v"(Bc[1]), "+v"(Bc[2]), "+v"(Bc[3]));
        else asm volatile("s_waitcnt vmcnt(8)" : "+v"(Bc[0]), "+v"(Bc[1]), "+v"(Bc[2]), "+v"(Bc[3]));
#pragma unroll
        for (int ks = 0; ks < 4; ++ks) {
            if (ks == 2) {
                if constexpr (c == 5) WAITV(12); else if constexpr (c == 6) WAITV(8); else WAITV(4);
                __builtin_amdgcn_s_barrier();
                SB0();
            }
#pragma unroll
            for (int t = 0; t < 7; ++t) {
                if (ks < 3) rdA(A[(ks + 1) & 1], b, ks + 1, t);
                else if constexpr (c < 7) rdA(A[0], bn, 0, t);
                acc[t] = mfma(A[ks & 1][t], __builtin_bit_cast(bf16x8, Bc[ks]), acc[t]);
                if constexpr (c + 2 < 8) { if (ks < 2 && (t == 1 || t == 4)) load_b(c + 2, 2 * ks + (t == 4), Bf[(BS + 2) % 3][2 * ks + (t == 4)]); }
                if (ks >= 2 && (t == 1 || t == 4)) {
                    const int p = 2 * (ks - 2) + (t == 4);
                    stage_kv_p(2 + 2 * (c - 5) + (p >> 1), p & 1);
                }
                SB0();
            }
        }
    };
    iterAT(integral_constant<int, 2>{}, integral_constant<int, 5>{});
    iterAT(integral_constant<int, 0>{}, integral_constant<int, 6>{});
    iterAT(integral_constant<int, 1>{}, integral_constant<int, 7>{});
    STAMP(0, 3);
    WAITV(4);
    BARRIER();
    stage_kv_p(8, 0); stage_kv_p(8, 1); stage_kv_p(9, 0); stage_kv_p(9, 1);
    bf16x8 qf[8];
    {
#pragma unroll
        for (int t = 4; t < 7; ++t) {
            f32x16 hv;
#pragma unroll
            for (int e = 0; e < 8; ++e) {
                const float g = acc[t][e], u = acc[t][8 + e];
                const float z = g * (1.f + 0.044715f * g * g) * (2.f * 0.7978845608028654f * 1.4426950408889634f);
                hv[e] = g * __builtin_amdgcn_rcpf(1.f + __builtin_amdgcn_exp2f(-z)) * u;
            }
            const int ft = head * 3 + (t - 4);
            if (ft < NFT) hfrag[((size_t)(qb * 8 + wave) * NFT + ft) * 64 + lane] = __builtin_bit_cast(u32x4, pack8(hv, 0));
        }
        const float qs = 0.08838834764831845f * 1.4426950408889634f;
#pragma unroll
        for (int t = 0; t < 4; ++t) {
#pragma unroll
            for (int e = 0; e < 16; ++e) acc[t][e] *= qs;
            qf[2 * t] = pack8(acc[t], 0);
            qf[2 * t + 1] = pack8(acc[t], 1);
        }
    }
    SB0();
    STAMP(0, 4);

    f32x16 o[4];
#pragma unroll
    for (int t = 0; t < 4; ++t) o[t] = f32x16{};
    float m, l = 0.f;
    f32x16 negm;
    const unsigned krd = r * 256 + ((hh ^ (r & 15)) * 16);
    const unsigned vrd = (hh * 4 + q4) * 64 + g16 * 32 + p4 * 8;
    auto rdK = [&](int j, int i) {
        const char* kb = lds + kv_slot(j == 0 ? 0 : 2 * j - 1);
        return row_frag(kb, (krd ^ ((i >> 1) * 32)) + (i & 1) * 8192);
    };
    auto rdV = [&](int j, int i) {
        const char* vb = lds + kv_slot(j == 7 ? 15 : 2 * j + 2);
        const unsigned a = vrd + (2 * (i >> 2) * 4 + (i & 3)) * 512;
        return tr_frag(vb, a, a + 2048);
    };
    constexpr int PD = 5;
    bf16x8 fr[PD + 1];
    f32x16 S[2][2];
    bf16x8 pf[4];
    {
#pragma unroll
        for (int i = 0; i < PD; ++i) fr[i % (PD + 1)] = rdK(0, i);
#pragma unroll
        for (int i = 0; i < 16; ++i) {
            if (i + PD < 16) fr[(i + PD) % (PD + 1)] = rdK(0, i + PD);
            else fr[(i + PD) % (PD + 1)] = rdK(1, i + PD - 16);
            S[0][i & 1] = mfma(fr[i % (PD + 1)], qf[i >> 1], i < 2 ? f32x16{} : S[0][i & 1]);
            SB0();
        }
        float pm = max3(S[0][0][0], S[0][0][1], S[0][1][0]);
#pragma unroll
        for (int e = 2; e < 16; ++e) pm = fmaxf(pm, S[0][0][e]);
#pragma unroll
        for (int e = 1; e < 16; ++e) pm = fmaxf(pm, S[0][1][e]);
        m = max32(pm);
#pragma unroll
        for (int e = 0; e < 16; ++e) { S[0][0][e] -= m; S[0][1][e] -= m; negm[e] = -m; }
    }
    auto iterB = [&](auto JC, auto DC) {
        constexpr int j = decltype(JC)::value;
        constexpr int dma0 = decltype(DC)::value;
        constexpr int c = j & 1, n = c ^ 1;
        constexpr int g0 = 16 + 32 * j;
        float ps0 = 0.f, ps1 = 0.f;
#pragma unroll
        for (int i = 0; i < 16; ++i) {
            if constexpr (j < 7) {
                if (i + PD < 16) fr[(g0 + i + PD) % (PD + 1)] = rdK(j + 1, i + PD);
                else fr[(g0 + i + PD) % (PD + 1)] = rdV(j, i + PD - 16);
                S[n][i & 1] = mfma(fr[(g0 + i) % (PD + 1)], qf[i >> 1], i < 2 ? negm : S[n][i & 1]);
            }
            S[c][0][i] = __builtin_amdgcn_exp2f(S[c][0][i]);
            if (i & 1) ps1 += S[c][0][i]; else ps0 += S[c][0][i];
            if ((i & 7) == 7) pf[i >> 3] = pack8(S[c][0], i >> 3);
            if constexpr (dma0 != 0) { if (i == 3 || i == 11) stage_kv_p(dma0, i >> 3); }
            SB0();
        }
        constexpr int g1 = (j < 7) ? g0 + 16 : g0;
        float pm = -3e38f;
#pragma unroll
        for (int i = 0; i < 16; ++i) {
            if (i + PD < 16) fr[(g1 + i + PD) % (PD + 1)] = rdV(j, i + PD);
            else if constexpr (j < 6) fr[(g1 + i + PD) % (PD + 1)] = rdK(j + 2, i + PD - 16);
            else if constexpr (j == 6) fr[(g1 + i + PD) % (PD + 1)] = rdV(7, i + PD - 16);
            if (i < 8) {
                S[c][1][2 * i] = __builtin_amdgcn_exp2f(S[c][1][2 * i]);
                S[c][1][2 * i + 1] = __builtin_amdgcn_exp2f(S[c][1][2 * i + 1]);
                ps0 += S[c][1][2 * i];
                ps1 += S[c][1][2 * i + 1];
                if (i == 3) pf[2] = pack8(S[c][1], 0);
                if (i == 7) pf[3] = pack8(S[c][1], 1);
            }
            o[i & 3] = mfma(fr[(g1 + i) % (PD + 1)], pf[i >> 2], o[i & 3]);
            if constexpr (j < 7) { if (i >= 8) { pm = max3(pm, S[n][(i - 8) >> 2][(4 * (i - 8)) & 15], S[n][(i - 8) >> 2][((4 * (i - 8)) & 15) + 1]); pm = max3(pm, S[n][(i - 8) >> 2][((4 * (i - 8)) & 15) + 2], S[n][(i - 8) >> 2][((4 * (i - 8)) & 15) + 3]); } }
            if constexpr (dma0 != 0 && dma0 + 1 <= 15) { if (i == 3 || i == 11) stage_kv_p(dma0 + 1, i >> 3); }
            SB0();
        }
        l += ps0 + ps1;
        if constexpr (j < 7) {
            pm = max32(pm);
            if (__builtin_expect(__any(pm > 8.f), 0)) {
                const float d = fmaxf(pm, 0.f);
                const float al = __builtin_amdgcn_exp2f(-d);
                m += d;
                l *= al;
#pragma unroll
                for (int e = 0; e < 16; ++e) { S[n][0][e] -= d; S[n][1][e] -= d; negm[e] = -m; }
#pragma unroll
                for (int t = 0; t < 4; ++t)
#pragma unroll
                    for (int e = 0; e < 16; ++e) o[t][e] *= al;
            }
        }
    };
#define RAWBAR() do { __builtin_amdgcn_s_barrier(); SB0(); } while (0)
    iterB(integral_constant<int, 0>{}, integral_constant<int, 0>{});
    STAMP(0, 5);
    RAWBAR();
    iterB(integral_constant<int, 1>{}, integral_constant<int, 10>{});
    STAMP(0, 6);
    WAITV(8); RAWBAR();
    iterB(integral_constant<int, 2>{}, integral_constant<int, 12>{});
    STAMP(0, 7);
    WAITV(8); RAWBAR();
    iterB(integral_constant<int, 3>{}, integral_constant<int, 14>{});
    unsigned pfd = 0;
    {
        const int sp2 = xcd & 3;
        int row = idx * 43 + wave * 8 + (lane >> 3);
        row = row < idx * 43 + 43 ? row : idx * 43 + 42;
        int grow = (sp2 < 3 ? sp2 * 1376 : 0) + row;
        grow = grow < (sp2 < 3 ? NH * DH : FF) ? grow : (sp2 < 3 ? NH * DH : FF) - 1;
        const char* pp = (const char*)(sp2 < 3 ? wo : wd) + (size_t)grow * (DM * 2) + (lane & 7) * 128;
        if (wave < 6) asm volatile("global_load_ushort %0, %1, off" : "=v"(pfd) : "v"(pp));
    }
    STAMP(0, 8);
    WAITV(8); RAWBAR();
    iterB(integral_constant<int, 4>{}, integral_constant<int, 0>{});
    STAMP(0, 9);
    WAITV(4); RAWBAR();
    iterB(integral_constant<int, 5>{}, integral_constant<int, 0>{});
    STAMP(0, 10);
    WAITV(0); RAWBAR();
    asm volatile("" ::"v"(pfd));
    iterB(integral_constant<int, 6>{}, integral_constant<int, 0>{});
    STAMP(0, 11);
    iterB(integral_constant<int, 7>{}, integral_constant<int, 0>{});
    STAMP(0, 12);

    l = sum32(l);
    const float rl = 1.f / l;
    const unsigned lane2 = lane_id_opaque();
#pragma unroll
    for (int t = 0; t < 4; ++t) {
#pragma unroll
        for (int e = 0; e < 16; ++e) o[t][e] *= rl;
        const __amdgpu_buffer_rsrc_t ors = __builtin_amdgcn_make_buffer_rsrc(ofrag, 0, 64 * 256 * 1024, 0x00020000);
        const unsigned off = (((qb * 8 + wave) * 256 + head * 8 + 2 * t) * 64 + lane2) * 16;
        __builtin_amdgcn_raw_buffer_store_b128(__builtin_bit_cast(u32x4, pack8(o[t], 0)), ors, off, 0, 16);
        __builtin_amdgcn_raw_buffer_store_b128(__builtin_bit_cast(u32x4, pack8(o[t], 1)), ors, off + 1024, 0, 16);
    }
    STAMP(0, 13);
    RSTAMP(0, 31);
    STAMP_FLUSH(0, 14);
}

constexpr unsigned K2_STG = 32768;
constexpr unsigned K2_LDS = 4 * K2_STG + 1024;

__global__ __launch_bounds__(512, 2) void k2_out(const __hip_bfloat16* __restrict__ x, const __hip_bfloat16* __restrict__ wo,
                                                 const __hip_bfloat16* __restrict__ wd, const u32x4* __restrict__ ofrag,
                                                 const u32x4* __restrict__ hfrag, float* __restrict__ part, unsigned* __restrict__ cnt,
                                                 __hip_bfloat16* __restrict__ out) {
    extern __shared__ __attribute__((aligned(16))) char lds[];
    const int tid = threadIdx.x, lane = tid & 63;
    const int wave = __builtin_amdgcn_readfirstlane(tid >> 6);
    const int r = lane & 31, hh = lane >> 5;
    const int bid = blockIdx.x, xcd = bid & 7, idx = bid >> 3;
    const int sp = xcd & 3, tq = (xcd >> 2) * 8 + (idx >> 2), tn = idx & 3;
    const int tile = tq * 4 + tn;
    const int g = wave & 3, kh = wave >> 2;
    STAMP_DECL;
    STAMP(1, 0);
    u32x2 xres[2];
    {
        const char* xp = (const char*)x + ((size_t)(tq * 128 + sp * 32 + r) * DM + tn * 128 + (wave >> 1) * 32 + 16 * (wave & 1) + 4 * hh) * 2;
        asm volatile("global_load_dwordx2 %0, %2, off\n\tglobal_load_dwordx2 %1, %2, off offset:16" : "=&v"(xres[0]), "=&v"(xres[1]) : "v"(xp));
    }
    const int s0 = sp < 3 ? sp * 86 : 0;
    const int ns = sp == 2 ? 84 : 86;
    const int ktot = sp < 3 ? NH * DH : FF;
    const char* wbase = (const char*)(sp < 3 ? wo : wd);
    const unsigned ldsb = (unsigned)(size_t)(LAS char*)lds;
    const char* bbase = (const char*)(sp < 3 ? ofrag + ((size_t)(tq * 4 + g) * 256 + s0) * 64 : hfrag + (size_t)(tq * 4 + g) * NFT * 64);
    const unsigned boff = lane * 16;
    const int wrow = s0 * 16 + wave * 16 + ((lane >> 2) & 7);
    const unsigned wcol = (tn * 128 + (lane >> 5) * 32 + (lane & 3) * 8) * 2;
    const int rot2 = 0;
    auto phys = [&](int c) { int q = c + rot2; q = q >= 10 ? q - 10 : q; return c < 10 ? q : 10; };
    auto stage_w = [&](int c, int half) {
        int r0 = wrow + phys(c) * 128 + half * 8;
        r0 = r0 < ktot ? r0 : ktot - 1;
        const unsigned a0 = r0 * (DM * 2) + wcol;
        const unsigned o_ = ldsb + (c & 3) * K2_STG + wave * 4096 + half * 2048;
        glds16s(wbase, a0, o_);
        glds16s(wbase + 128, a0, o_ + 1024);
    };
    f32x16 acc[4];
#pragma unroll
    for (int t = 0; t < 4; ++t) acc[t] = f32x16{};
    const int q4 = (lane & 15) >> 2, p4 = lane & 3, g16 = (lane >> 4) & 1;
    const unsigned trd = (hh * 4 + q4) * 64 + g16 * 32 + p4 * 8 + kh * 4096;
    u32x4 bf[4][4];
    auto load_b = [&](int c, int i) {
        int s = 8 * phys(c) + 2 * i + kh;
        s = s < ns ? s : ns - 1;
        const char* sb = bbase + (size_t)s * 1024;
        asm volatile("s_nop 4\n\tglobal_load_dwordx4 %0, %1, %2" : "=v"(bf[c & 3][i]) : "v"(boff), "s"(sb));
    };
    auto rdW = [&](int c, int i) {
        const unsigned a = trd + (i >> 2) * 8192 + (i & 3) * 512;
        return tr_frag(lds + (c & 3) * K2_STG, a, a + 2048);
    };
    constexpr int PD = 6;
    bf16x8 fr[PD + 1];
#pragma unroll
    for (int c = 0; c < 3; ++c) {
        stage_w(c, 0); stage_w(c, 1);
        load_b(c, 0); load_b(c, 1); load_b(c, 2); load_b(c, 3);
    }
    auto stage_iter = [&](auto CC) {
        constexpr int c = decltype(CC)::value;
        u32x4(&b)[4] = bf[c & 3];
        if constexpr (c == 0) asm volatile("s_waitcnt vmcnt(16)" : "+v"(b[0]), "+v"(b[1]), "+v"(b[2]), "+v"(b[3]), "+v"(xres[0]), "+v"(xres[1])::"memory");
        else if constexpr (c < 9) asm volatile("s_waitcnt vmcnt(16)" : "+v"(b[0]), "+v"(b[1]), "+v"(b[2]), "+v"(b[3])::"memory");
        else if constexpr (c == 9) asm volatile("s_waitcnt vmcnt(8)" : "+v"(b[0]), "+v"(b[1]), "+v"(b[2]), "+v"(b[3])::"memory");
        else asm volatile("s_waitcnt vmcnt(0)" : "+v"(b[0]), "+v"(b[1]), "+v"(b[2]), "+v"(b[3])::"memory");
        BARRIER();
        STAMP(1, 1 + c);
#pragma unroll
        for (int i = 0; i < PD; ++i) fr[i % (PD + 1)] = rdW(c, i);
#pragma unroll
        for (int i = 0; i < 16; ++i) {
            if (i + PD < 16) fr[(i + PD) % (PD + 1)] = rdW(c, i + PD);
            if (c < 10 || 8 * c + 2 * (i >> 2) + kh < ns) acc[i & 3] = mfma(fr[i % (PD + 1)], __builtin_bit_cast(bf16x8, b[i >> 2]), acc[i & 3]);
            if constexpr (c + 3 < 11) {
                if (i == 0) stage_w(c + 3, 0);
                if (i == 2) stage_w(c + 3, 1);
                if (i == 4) load_b(c + 3, 0);
                if (i == 6) load_b(c + 3, 1);
                if (i == 8) load_b(c + 3, 2);
                if (i == 10) load_b(c + 3, 3);
            }
            SB0();
        }
    };
    using std::integral_constant;
    stage_iter(integral_constant<int, 0>{});
    stage_iter(integral_constant<int, 1>{});
    stage_iter(integral_constant<int, 2>{});
    stage_iter(integral_constant<int, 3>{});
    stage_iter(integral_constant<int, 4>{});
    stage_iter(integral_constant<int, 5>{});
    stage_iter(integral_constant<int, 6>{});
    stage_iter(integral_constant<int, 7>{});
    stage_iter(integral_constant<int, 8>{});
    stage_iter(integral_constant<int, 9>{});
    stage_iter(integral_constant<int, 10>{});
    BARRIER();
    STAMP(1, 12);
    if (kh == 1) {
#pragma unroll
        for (int t = 0; t < 4; ++t)
#pragma unroll
            for (int qd = 0; qd < 4; ++qd) {
                f32x4 v = {acc[t][4 * qd], acc[t][4 * qd + 1], acc[t][4 * qd + 2], acc[t][4 * qd + 3]};
                *(f32x4*)(lds + g * 16384 + (t * 4 + qd) * 1024 + lane * 16) = v;
            }
    }
    BARRIER();
    const __amdgpu_buffer_rsrc_t prs = __builtin_amdgcn_make_buffer_rsrc(part, 0, 64 * 4 * 65536, 0x00020000);
    if (kh == 0) {
#pragma unroll
        for (int t = 0; t < 4; ++t)
#pragma unroll
            for (int qd = 0; qd < 4; ++qd) {
                f32x4 v = *(const f32x4*)(lds + g * 16384 + (t * 4 + qd) * 1024 + lane * 16);
                v[0] += acc[t][4 * qd]; v[1] += acc[t][4 * qd + 1]; v[2] += acc[t][4 * qd + 2]; v[3] += acc[t][4 * qd + 3];
                if (g != sp) __builtin_amdgcn_raw_buffer_store_b128(__builtin_bit_cast(u32x4, v), prs, ((tile * 4 + sp) * 4 + g) * 16384 + (t * 4 + qd) * 1024 + lane * 16, 0, 16);
                else *(f32x4*)(lds + 65536 + (t * 4 + qd) * 1024 + lane * 16) = v;
            }
    }
    STAMP(1, 13);
    WAITV(0);
    BARRIER();
    STAMP(1, 14);
    if (tid == 0) {
        __hip_atomic_fetch_add((GAS unsigned*)(cnt + tile * 16), 1u, __ATOMIC_RELAXED, __HIP_MEMORY_SCOPE_AGENT);
        unsigned spins = 0;
        while (__hip_atomic_load((GAS unsigned*)(cnt + tile * 16), __ATOMIC_RELAXED, __HIP_MEMORY_SCOPE_AGENT) < 4u) {
            __builtin_amdgcn_s_sleep(1);
            if (++spins > (1u << 22)) break;
        }
    }
    BARRIER();
    STAMP(1, 15);
    {
        const int t = wave >> 1, q0 = 2 * (wave & 1);
        const int pi = t * 4 + q0;
        f32x4 f[3][2];
#pragma unroll
        for (int k = 0; k < 3; ++k) {
            const int src = k + (k >= sp ? 1 : 0);
#pragma unroll
            for (int qd = 0; qd < 2; ++qd)
                f[k][qd] = __builtin_bit_cast(f32x4, __builtin_amdgcn_raw_buffer_load_b128(prs, ((tile * 4 + src) * 4 + sp) * 16384 + (pi + qd) * 1024 + lane * 16, 0, 16));
        }
        const int row = tq * 128 + sp * 32 + r;
#pragma unroll
        for (int qd = 0; qd < 2; ++qd) {
            const f32x4 own = *(const f32x4*)(lds + 65536 + (pi + qd) * 1024 + lane * 16);
            const f32x4 sv = (f[0][qd] + f[1][qd]) + (sp == 3 ? f[2][qd] : own);
            const f32x4 gv = sp == 3 ? own : f[2][qd];
            const int n = tn * 128 + t * 32 + 8 * (q0 + qd) + 4 * hh;
            const u32x2 xv = xres[qd];
            float xf[4] = {__uint_as_float(xv[0] << 16), __uint_as_float(xv[0] & 0xffff0000u), __uint_as_float(xv[1] << 16), __uint_as_float(xv[1] & 0xffff0000u)};
            float ov[4];
#pragma unroll
            for (int e = 0; e < 4; ++e) {
                const float s = bfr(sv[e]);
                const float gg = bfr(gv[e]);
                const float sg = bfr(gg * __builtin_amdgcn_rcpf(1.f + __builtin_amdgcn_exp2f(-gg * 1.4426950408889634f)));
                ov[e] = xf[e] + bfr(s * sg);
            }
            u32x2 w = {cvtpk(ov[0], ov[1]), cvtpk(ov[2], ov[3])};
            *(u32x2*)((char*)out + ((size_t)row * DM + n) * 2) = w;
        }
    }
    STAMP(1, 16);
    STAMP(1, 17);
    STAMP_FLUSH(1, 18);
}

extern "C" void kernel_launch(void* const* d_in, const int* in_sizes, int n_in, void* d_out, int out_size, void* d_ws, size_t ws_size,
                              hipStream_t stream) {
    const __hip_bfloat16* x = (const __hip_bfloat16*)d_in[0];
    const __hip_bfloat16* kc = (const __hip_bfloat16*)d_in[1];
    const __hip_bfloat16* vc = (const __hip_bfloat16*)d_in[2];
    const __hip_bfloat16* wq = (const __hip_bfloat16*)d_in[3];
    const __hip_bfloat16* wo = (const __hip_bfloat16*)d_in[4];
    const __hip_bfloat16* wg = (const __hip_bfloat16*)d_in[6];
    const __hip_bfloat16* wu = (const __hip_bfloat16*)d_in[7];
    const __hip_bfloat16* wd = (const __hip_bfloat16*)d_in[8];
    char* ws = (char*)d_ws;
    u32x4* ofrag = (u32x4*)ws;
    u32x4* hfrag = (u32x4*)(ws + (16u << 20));
    float* part = (float*)(ws + (24u << 20));
    unsigned* cnt = (unsigned*)(ws + (40u << 20));
    u32x4* xfrag = (u32x4*)(ws + (41u << 20));
    (void)hipFuncSetAttribute((const void*)k1_attn<true>, hipFuncAttributeMaxDynamicSharedMemorySize, K1_LDS);
    (void)hipFuncSetAttribute((const void*)k1_attn<false>, hipFuncAttributeMaxDynamicSharedMemorySize, K1_LDS);
    (void)hipFuncSetAttribute((const void*)k2_out, hipFuncAttributeMaxDynamicSharedMemorySize, K2_LDS);
    k0_ln<<<256, 256, 0, stream>>>(x, xfrag, cnt, wq, wg, wu);
    const long long dgu = (const char*)wu - (const char*)wg;
    if (dgu > -(1ll << 31) && dgu < (1ll << 31)) k1_attn<true><<<256, 512, K1_LDS, stream>>>(xfrag, kc, vc, wq, wg, wu, ofrag, hfrag, wo, wd);
    else k1_attn<false><<<256, 512, K1_LDS, stream>>>(xfrag, kc, vc, wq, wg, wu, ofrag, hfrag, wo, wd);
    k2_out<<<256, 512, K2_LDS, stream>>>(x, wo, wd, ofrag, hfrag, part, cnt, (__hip_bfloat16*)d_out);
}
```
